```python
import torch

def _softmax(x, axis=-1):
    return torch.softmax(x, dim=axis)
def _gelu(x):
    return torch.nn.functional.gelu(x, approximate="tanh")
def _silu(x):
    return torch.nn.functional.silu(x)
def _sigmoid(x):
    return torch.sigmoid(x)
def _layernorm(x, axis=-1, eps=1e-5):
    m = x.mean(dim=axis, keepdim=True)
    v = x.var(dim=axis, keepdim=True, unbiased=False)
    return (x - m) / torch.sqrt(v + eps)
def _scatter_add(tgt, idx, src, axis=0):
    return tgt.clone().index_add_(axis, idx, src)
def _segment_sum(src, seg, n):
    out = torch.zeros((n,) + src.shape[1:], dtype=src.dtype)
    return out.index_add_(0, seg, src)
def _pad(x, pw):
    flat = tuple(v for pair in reversed(pw) for v in pair)
    return torch.nn.functional.pad(x, flat)

kernel_name = 'standalone__modern_conv_block_slak__vision'

def setup_inputs():

    _w = (lambda a: (lambda t: t.to(torch.bfloat16) if t.is_floating_point() else t)(torch.as_tensor(a)))
    H = W = 224
    C = 96
    K = 7
    return {
        "x": _w((lambda *s: torch.randn(*s, dtype=torch.bfloat16) * 0.05)(1, H, W, C)),
        "dw": _w((lambda *s: torch.randn(*s, dtype=torch.bfloat16) * 0.05)(K, K, C)),
        "w1": _w((lambda *s: torch.randn(*s, dtype=torch.bfloat16) * 0.05)(C, 4 * C)),
        "w2": _w((lambda *s: torch.randn(*s, dtype=torch.bfloat16) * 0.05)(4 * C, C)),
        "grn_g": _w((lambda *s: torch.randn(*s, dtype=torch.bfloat16) * 0.05)(4 * C)),
        "grn_b": _w((lambda *s: torch.randn(*s, dtype=torch.bfloat16) * 0.05)(4 * C)),
    }

def reference(x, dw, w1, w2, grn_g, grn_b):
    B, H, W, C = x.shape
    K = dw.shape[0]
    pad = K // 2
    xp = _pad(x, ((0,0),(pad,pad),(pad,pad),(0,0)))
    out = torch.zeros_like(x)
    for i in range(K):
        for j in range(K):
            out = out + xp[:, i:i+H, j:j+W, :] * dw[i, j][None, None, None, :]
    h = _layernorm(out, axis=-1)
    h = _gelu(h @ w1)
    gx = torch.sqrt((h * h).sum(axis=(1, 2), keepdims=True) + 1e-6)
    nx = gx / (gx.mean(axis=-1, keepdims=True) + 1e-6)
    h = grn_g[None, None, None, :] * (h * nx) + grn_b[None, None, None, :] + h
    return x + h @ w2
```

```cpp
#include <hip/hip_runtime.h>
#include <hip/hip_bf16.h>

constexpr unsigned LDS_BYTES = 163840;

__global__ void __launch_bounds__(512, 2)
convblock_kernel(const void* x, const void* dw, const void* w1, const void* w2, const void* gg, const void* gb, void* out, void* ws) {
  unsigned tid = threadIdx.x;
  asm volatile(
    "s_load_dwordx2 s[4:5], s[0:1], 8\n\t"
    "s_load_dwordx2 s[20:21], s[0:1], 0\n\t"
    "s_getpc_b64 s[56:57]\n\t"
    "v_lshlrev_b32 v12, 7, v0\n\t"
    "v_cmp_gt_u32 vcc, 23040, v12\n\t"
    "s_and_saveexec_b64 s[78:79], vcc\n\t"
    "s_cbranch_execz 29f\n\t"
    "global_load_dword v250, v12, s[56:57]\n\t"
    "29:\n\t"
    "s_mov_b64 exec, -1\n\t"
    "s_setprio 3\n\t"
    "s_mov_b32 s6, 9408\n\t"
    "s_mov_b32 s7, 0x20000\n\t"
    "v_readfirstlane_b32 s36, v0\n\t"
    "v_mul_u32_u24 v1, 0x556, v0\n\t"
    "v_lshrrev_b32 v8, 16, v1\n\t"
    "v_mul_u32_u24 v1, 1152, v8\n\t"
    "v_lshl_add_u32 v1, v0, 2, v1\n\t"
    "v_and_b32 v9, 63, v0\n\t"
    "s_lshr_b32 s36, s36, 6\n\t"
    "s_mov_b32 s22, 0x930000\n\t"
    "s_mov_b32 s23, s7\n\t"
    "s_lshr_b32 s38, s36, 2\n\t"
    "s_and_b32 s3, s36, 3\n\t"
    "s_lshl_b32 s78, s2, 1\n\t"
    "s_and_b32 s78, s78, 12\n\t"
    "s_lshr_b32 s79, s2, 6\n\t"
    "s_add_u32 s78, s78, s79\n\t"
    "s_mul_i32 s78, s78, 14\n\t"
    "s_and_b32 s77, s2, 1\n\t"
    "s_lshl_b32 s77, s77, 3\n\t"
    "s_bfe_u32 s79, s2, 0x30003\n\t"
    "s_or_b32 s77, s77, s79\n\t"
    "s_mul_i32 s77, s77, 14\n\t"
    "v_mul_u32_u24 v10, 43, v9\n\t"
    "v_lshrrev_b32 v10, 9, v10\n\t"
    "v_mul_u32_u24 v11, 12, v10\n\t"
    "v_sub_u32 v11, v9, v11\n\t"
    "s_mul_i32 s79, s3, 5\n\t"
    "v_add_u32 v10, s79, v10\n\t"
    "v_lshrrev_b32 v12, 2, v10\n\t"
    "v_add_u32 v12, s38, v12\n\t"
    "v_and_b32 v12, 3, v12\n\t"
    "v_xor_b32 v11, v11, v12\n\t"
    "s_sub_u32 s79, s77, 3\n\t"
    "v_add_u32 v10, s79, v10\n\t"
    "v_cmp_gt_u32 vcc, 0xe0, v10\n\t"
    "v_mul_u32_u24 v10, 0xc0, v10\n\t"
    "v_lshl_add_u32 v10, v11, 4, v10\n\t"
    "v_xor_b32 v11, 32, v10\n\t"
    "v_cndmask_b32 v10, 2.0, v10, vcc\n\t"
    "v_cndmask_b32 v11, 2.0, v11, vcc\n\t"
    "s_add_u32 s79, s78, s38\n\t"
    "s_sub_u32 s79, s79, 3\n\t"
    "s_mul_i32 s59, s79, 43008\n\t"
    "s_mul_i32 s55, s36, 960\n\t"
    "s_waitcnt lgkmcnt(0)\n\t"
    "buffer_load_dword v16, v1, s[4:7], 0 offen offset:0\n\t"
    "buffer_load_dword v17, v1, s[4:7], 0 offen offset:192\n\t"
    "buffer_load_dword v18, v1, s[4:7], 0 offen offset:384\n\t"
    "buffer_load_dword v19, v1, s[4:7], 0 offen offset:576\n\t"
    "buffer_load_dword v20, v1, s[4:7], 0 offen offset:768\n\t"
    "buffer_load_dword v21, v1, s[4:7], 0 offen offset:960\n\t"
    "buffer_load_dword v22, v1, s[4:7], 0 offen offset:1152\n\t"
    "s_load_dwordx2 s[8:9], s[0:1], 16\n\t"
    "s_load_dwordx2 s[12:13], s[0:1], 24\n\t"
    "s_load_dwordx4 s[28:31], s[0:1], 32\n\t"
    "s_load_dwordx2 s[24:25], s[0:1], 48\n\t"
    "s_load_dwordx2 s[16:17], s[0:1], 56\n\t"
    "s_add_u32 m0, s55, 0\n\t"
    "s_add_u32 s39, s59, 0\n\t"
    "s_cmp_ge_i32 s79, 0\n\t"
    "s_cselect_b32 s39, s39, 2.0\n\t"
    "s_mov_b32 exec_hi, 0x0fffffff\n\t"
    "buffer_load_dwordx4 v10, s[20:23], s39 offen lds\n\t"
    "s_mov_b32 exec_hi, -1\n\t"
    "s_add_u32 m0, s55, 7680\n\t"
    "s_add_u32 s39, s59, 86016\n\t"
    "s_cmp_ge_i32 s79, -2\n\t"
    "s_cselect_b32 s39, s39, 2.0\n\t"
    "s_mov_b32 exec_hi, 0x0fffffff\n\t"
    "buffer_load_dwordx4 v11, s[20:23], s39 offen lds\n\t"
    "s_mov_b32 exec_hi, -1\n\t"
    "s_mul_i32 s43, s78, 43008\n\t"
    "s_mul_i32 s77, s77, 192\n\t"
    "s_add_u32 s43, s43, s77\n\t"
    "s_mov_b32 s42, s2\n\t"
    "s_mov_b32 s10, 73728\n\t"
    "s_mov_b32 s11, s7\n\t"
    "s_mov_b32 s14, 73728\n\t"
    "s_mov_b32 s15, s7\n\t"
    "s_mov_b32 s18, 823296\n\t"
    "s_mov_b32 s19, s7\n\t"
    "s_mov_b32 s26, 9633792\n\t"
    "s_mov_b32 s27, s7\n\t"
    "s_mov_b32 s44, 0x22221111\n\t"
    "s_mov_b32 s45, 0x88884444\n\t"
    "s_mov_b32 s46, 0x88884444\n\t"
    "s_mov_b32 s47, 0x22221111\n\t"
    "s_mov_b32 s48, 0x2aaa1555\n\t"
    "s_mov_b32 s49, 0x8aaa4555\n\t"
    "s_mov_b32 s80, 0x11112222\n\t"
    "s_mov_b32 s81, 0x44448888\n\t"
    "s_mov_b32 s82, 0x44448888\n\t"
    "s_mov_b32 s83, 0x11112222\n\t"
    "s_mov_b32 s84, 0x3fff3fff\n\t"
    "s_mov_b32 s85, 0xcfffcfff\n\t"
    "s_add_u32 m0, s55, 15360\n\t"
    "s_add_u32 s39, s59, 172032\n\t"
    "s_mov_b32 exec_hi, 0x0fffffff\n\t"
    "buffer_load_dwordx4 v10, s[20:23], s39 offen lds\n\t"
    "s_mov_b32 exec_hi, -1\n\t"
    "s_mov_b32 s50, 0xc09d30fc\n\t"
    "s_mov_b32 s51, s50\n\t"
    "s_mov_b32 s52, 0x3c2aaaab\n\t"
    "s_lshl_b32 s32, s36, 10\n\t"
    "s_lshl_b32 s33, s3, 10\n\t"
    "s_lshl_b32 s34, s36, 5\n\t"
    "s_lshl_b32 s35, s3, 5\n\t"
    "s_mul_i32 s53, s36, 9216\n\t"
    "s_mov_b32 s54, 0xffff\n\t"
    "s_lshr_b32 s37, 0xc3, s36\n\t"
    "s_and_b32 s37, s37, 1\n\t"
    "v_mov_b32 v24, 0\n\t"
    "v_mov_b32 v25, 0\n\t"
    "v_mov_b32 v26, 0\n\t"
    "v_mov_b32 v27, 0\n\t"
    "v_lshlrev_b32 v1, 4, v0\n\t"
    "v_add_u32 v1, 121984, v1\n\t"
    "v_cmp_lt_u32 vcc, 0x137, v0\n\t"
    "v_lshrrev_b32 v12, 4, v9\n\t"
    "v_bfe_u32 v13, v9, 2, 2\n\t"
    "s_mov_b64 exec, vcc\n\t"
    "ds_write_b128 v1, v[24:27]\n\t"
    "ds_write_b128 v1, v[24:27] offset:25600\n\t"
    "s_mov_b64 exec, 7\n\t"
    "v_mul_u32_u24 v1, 272, v9\n\t"
    "v_add_u32 v1, 88320, v1\n\t"
    "ds_write_b128 v1, v[24:27]\n\t"
    "s_mov_b64 exec, -1\n\t"
    "s_setprio 2\n\t"
    "s_add_u32 m0, s55, 23040\n\t"
    "s_add_u32 s39, s59, 258048\n\t"
    "s_mov_b32 exec_hi, 0x0fffffff\n\t"
    "buffer_load_dwordx4 v11, s[20:23], s39 offen lds\n\t"
    "s_mov_b32 exec_hi, -1\n\t"
    "v_and_b32 v14, 3, v9\n\t"
    "v_lshrrev_b32 v15, 2, v9\n\t"
    "v_mul_u32_u24 v23, 2432, v8\n\t"
    "v_lshl_add_u32 v23, v0, 4, v23\n\t"
    "v_add_u32 v23, 130176, v23\n\t"
    "v_lshrrev_b32 v1, 5, v9\n\t"
    "v_bfe_u32 v2, v9, 1, 1\n\t"
    "v_sub_u32 v2, v1, v2\n\t"
    "v_mul_i32_i24 v72, 3200, v2\n\t"
    "v_bfe_u32 v3, v9, 4, 1\n\t"
    "v_lshrrev_b32 v2, 1, v9\n\t"
    "v_and_or_b32 v2, v2, 2, v3\n\t"
    "v_mul_u32_u24 v2, 800, v2\n\t"
    "v_and_b32 v4, 1, v9\n\t"
    "v_lshl_add_u32 v2, v4, 3, v2\n\t"
    "v_and_b32 v5, 8, v9\n\t"
    "v_lshl_add_u32 v2, v5, 1, v2\n\t"
    "s_mul_i32 s78, s36, 96\n\t"
    "s_add_u32 s78, s78, 130176\n\t"
    "v_add3_u32 v72, v72, v2, s78\n\t"
    "s_add_u32 m0, s55, 30720\n\t"
    "s_add_u32 s39, s59, 344064\n\t"
    "s_mov_b32 exec_hi, 0x0fffffff\n\t"
    "buffer_load_dwordx4 v10, s[20:23], s39 offen lds\n\t"
    "s_mov_b32 exec_hi, -1\n\t"
    "v_and_b32 v5, 4, v9\n\t"
    "v_mul_u32_u24 v5, 800, v5\n\t"
    "v_sub_u32 v73, v72, v5\n\t"
    "v_add_u32 v73, 1600, v73\n\t"
    "v_lshl_add_u32 v2, v14, 1, v13\n\t"
    "v_lshl_add_u32 v2, v3, 2, v2\n\t"
    "v_lshrrev_b32 v5, 2, v2\n\t"
    "v_add_u32 v5, v5, v1\n\t"
    "v_and_b32 v5, 3, v5\n\t"
    "v_mad_u32_u24 v2, v1, 20, v2\n\t"
    "v_mul_u32_u24 v2, 0xc0, v2\n\t"
    "v_lshl_add_u32 v2, v5, 4, v2\n\t"
    "s_mul_i32 s77, s36, 24\n\t"
    "s_add_u32 s78, s77, 0\n\t"
    "s_and_b32 s79, s78, 0x38\n\t"
    "s_andn2_b32 s78, s78, 63\n\t"
    "v_xor_b32 v76, s79, v2\n\t"
    "v_add_u32 v76, s78, v76\n\t"
    "v_xor_b32 v77, 32, v76\n\t"
    "s_add_u32 s78, s77, 8\n\t"
    "s_and_b32 s79, s78, 0x38\n\t"
    "s_andn2_b32 s78, s78, 63\n\t"
    "v_xor_b32 v78, s79, v2\n\t"
    "v_add_u32 v78, s78, v78\n\t"
    "s_add_u32 m0, s55, 38400\n\t"
    "s_add_u32 s39, s59, 430080\n\t"
    "s_mov_b32 exec_hi, 0x0fffffff\n\t"
    "buffer_load_dwordx4 v11, s[20:23], s39 offen lds\n\t"
    "s_mov_b32 exec_hi, -1\n\t"
    "v_xor_b32 v79, 32, v78\n\t"
    "s_add_u32 s78, s77, 16\n\t"
    "s_and_b32 s79, s78, 0x38\n\t"
    "s_andn2_b32 s78, s78, 63\n\t"
    "v_xor_b32 v80, s79, v2\n\t"
    "v_add_u32 v80, s78, v80\n\t"
    "v_xor_b32 v81, 32, v80\n\t"
    "v_xor_b32 v5, v12, v14\n\t"
    "v_cmp_eq_u32 vcc, 2, v5\n\t"
    "v_mul_u32_u24 v75, 416, v14\n\t"
    "v_lshl_add_u32 v75, v13, 3, v75\n\t"
    "v_cndmask_b32 v5, 0, 32, vcc\n\t"
    "s_mul_i32 s78, s36, 4992\n\t"
    "v_add3_u32 v75, v75, v5, s78\n\t"
    "v_xor_b32 v5, v12, v14\n\t"
    "v_and_b32 v6, 1, v5\n\t"
    "v_mul_u32_u24 v6, 1632, v6\n\t"
    "v_and_b32 v7, 3, v5\n\t"
    "v_cmp_eq_u32 vcc, 3, v7\n\t"
    "v_add_u32 v74, v75, v6\n\t"
    "v_lshlrev_b32 v6, 5, v5\n\t"
    "v_cndmask_b32 v7, 0, 32, vcc\n\t"
    "v_and_b32 v6, 32, v6\n\t"
    "v_add3_u32 v74, v74, v6, v7\n\t"
    "s_setprio 1\n\t"
    "s_add_u32 m0, s55, 46080\n\t"
    "s_add_u32 s39, s59, 516096\n\t"
    "s_mov_b32 exec_hi, 0x0fffffff\n\t"
    "buffer_load_dwordx4 v10, s[20:23], s39 offen lds\n\t"
    "s_mov_b32 exec_hi, -1\n\t"
    "v_lshrrev_b32 v85, 1, v9\n\t"
    "v_xor_b32 v5, v85, v12\n\t"
    "v_and_b32 v5, 3, v5\n\t"
    "v_lshrrev_b32 v82, 3, v9\n\t"
    "v_mul_u32_u24 v82, 0x300, v82\n\t"
    "v_lshl_add_u32 v82, v5, 5, v82\n\t"
    "v_lshl_add_u32 v82, v4, 4, v82\n\t"
    "v_mul_u32_u24 v83, 416, v15\n\t"
    "v_lshl_add_u32 v83, v14, 3, v83\n\t"
    "v_mul_u32_u24 v84, 416, v85\n\t"
    "v_lshl_add_u32 v84, v4, 4, v84\n\t"
    "v_lshlrev_b32 v106, 6, v12\n\t"
    "v_add_u32 v106, 88064, v106\n\t"
    "v_lshl_add_u32 v106, v14, 4, v106\n\t"
    "v_mov_b32 v85, v106\n\t"
    "v_cmp_eq_u32 vcc, v12, v13\n\t"
    "v_lshlrev_b32 v86, v14, 1\n\t"
    "v_xor_b32 v5, v13, v12\n\t"
    "v_cndmask_b32 v86, 0, v86, vcc\n\t"
    "v_and_b32 v5, 1, v5\n\t"
    "v_or_b32 v5, v5, v14\n\t"
    "v_cmp_eq_u32 vcc, 0, v5\n\t"
    "v_mov_b32 v87, 0x3f803f80\n\t"
    "v_lshlrev_b32 v100, 7, v15\n\t"
    "s_add_u32 m0, s55, 53760\n\t"
    "s_add_u32 s39, s59, 602112\n\t"
    "s_mov_b32 exec_hi, 0x0fffffff\n\t"
    "buffer_load_dwordx4 v11, s[20:23], s39 offen lds\n\t"
    "s_mov_b32 exec_hi, -1\n\t"
    "v_cndmask_b32 v87, 0, v87, vcc\n\t"
    "v_lshl_add_u32 v100, v14, 3, v100\n\t"
    "v_bfe_u32 v5, v9, 3, 2\n\t"
    "v_lshl_add_u32 v100, v5, 5, v100\n\t"
    "v_xor_b32 v101, 32, v100\n\t"
    "s_add_u32 s78, s32, 73728\n\t"
    "v_lshl_add_u32 v5, v12, 2, v9\n\t"
    "v_and_b32 v5, 15, v5\n\t"
    "v_and_or_b32 v5, v9, 48, v5\n\t"
    "v_lshl_add_u32 v104, v5, 4, s78\n\t"
    "v_add_u32 v5, v1, v14\n\t"
    "v_lshl_add_u32 v5, v5, 2, v13\n\t"
    "v_and_b32 v5, 15, v5\n\t"
    "v_lshl_add_u32 v5, v14, 4, v5\n\t"
    "v_lshl_add_u32 v105, v5, 4, s78\n\t"
    "v_lshl_add_u32 v105, v3, 3, v105\n\t"
    "s_lshl_b32 s78, s3, 1\n\t"
    "s_add_u32 s78, s78, 81920\n\t"
    "v_and_b32 v5, 31, v9\n\t"
    "v_lshl_add_u32 v107, v5, 3, s78\n\t"
    "v_add_u32 v5, 0, v9\n\t"
    "v_mul_u32_u24 v6, 0xab, v5\n\t"
    "v_lshrrev_b32 v6, 11, v6\n\t"
    "v_lshrrev_b32 v7, 1, v6\n\t"
    "s_add_u32 m0, s55, 61440\n\t"
    "s_add_u32 s39, s59, 688128\n\t"
    "s_mov_b32 exec_hi, 0x0fffffff\n\t"
    "buffer_load_dwordx4 v10, s[20:23], s39 offen lds\n\t"
    "s_mov_b32 exec_hi, -1\n\t"
    "v_and_b32 v7, 2, v7\n\t"
    "v_mul_u32_u24 v6, 12, v6\n\t"
    "v_sub_u32 v5, v5, v6\n\t"
    "v_xor_b32 v5, v5, v7\n\t"
    "v_add_lshl_u32 v252, v5, v6, 4\n\t"
    "v_add_u32 v5, 64, v9\n\t"
    "v_mul_u32_u24 v6, 0xab, v5\n\t"
    "v_lshrrev_b32 v6, 11, v6\n\t"
    "v_lshrrev_b32 v7, 1, v6\n\t"
    "v_and_b32 v7, 2, v7\n\t"
    "v_mul_u32_u24 v6, 12, v6\n\t"
    "v_sub_u32 v5, v5, v6\n\t"
    "v_xor_b32 v5, v5, v7\n\t"
    "v_add_lshl_u32 v253, v5, v6, 4\n\t"
    "v_add_u32 v5, 128, v9\n\t"
    "v_mul_u32_u24 v6, 0xab, v5\n\t"
    "v_lshrrev_b32 v6, 11, v6\n\t"
    "v_lshrrev_b32 v7, 1, v6\n\t"
    "v_and_b32 v7, 2, v7\n\t"
    "v_mul_u32_u24 v6, 12, v6\n\t"
    "v_sub_u32 v5, v5, v6\n\t"
    "v_xor_b32 v5, v5, v7\n\t"
    "v_add_lshl_u32 v254, v5, v6, 4\n\t"
    "s_lshl_b32 s78, s36, 2\n\t"
    "s_setprio 0\n\t"
    "s_add_u32 m0, s55, 69120\n\t"
    "s_add_u32 s39, s59, 774144\n\t"
    "s_mov_b32 exec_hi, 0x0fffffff\n\t"
    "buffer_load_dwordx4 v11, s[20:23], s39 offen lds\n\t"
    "s_mov_b32 exec_hi, -1\n\t"
    "s_lshr_b32 s78, 0x20545420, s78\n\t"
    "s_and_b32 s78, s78, 7\n\t"
    "s_lshl_b32 s78, s78, 2\n\t"
    "s_mov_b32 s56, 0x10543210\n\t"
    "s_mov_b32 s57, 0x5432\n\t"
    "s_lshr_b64 s[56:57], s[56:57], s78\n\t"
    "s_mul_i32 s79, s38, 6\n\t"
    "s_bfe_u32 s66, s56, 0x30000\n\t"
    "s_add_u32 s66, s66, s79\n\t"
    "s_mul_i32 s60, s66, 6144\n\t"
    "s_bitcmp1_b32 s66, 0\n\t"
    "s_cselect_b32 s78, -6080, 0\n\t"
    "s_add_u32 s60, s60, s78\n\t"
    "s_add_u32 s60, s60, 90112\n\t"
    "s_bfe_u32 s67, s56, 0x30004\n\t"
    "s_add_u32 s67, s67, s79\n\t"
    "s_mul_i32 s61, s67, 6144\n\t"
    "s_bitcmp1_b32 s67, 0\n\t"
    "s_cselect_b32 s78, -6080, 0\n\t"
    "s_add_u32 s61, s61, s78\n\t"
    "s_add_u32 s61, s61, 90112\n\t"
    "s_bfe_u32 s68, s56, 0x30008\n\t"
    "s_add_u32 s68, s68, s79\n\t"
    "s_mul_i32 s62, s68, 6144\n\t"
    "s_bitcmp1_b32 s68, 0\n\t"
    "s_cselect_b32 s78, -6080, 0\n\t"
    "s_add_u32 s62, s62, s78\n\t"
    "s_add_u32 s62, s62, 90112\n\t"
    "s_bfe_u32 s69, s56, 0x3000c\n\t"
    "s_add_u32 s69, s69, s79\n\t"
    "s_mul_i32 s63, s69, 6144\n\t"
    "s_bitcmp1_b32 s69, 0\n\t"
    "s_cselect_b32 s78, -6080, 0\n\t"
    "s_add_u32 s63, s63, s78\n\t"
    "s_add_u32 s63, s63, 90112\n\t"
    "s_bfe_u32 s70, s56, 0x30010\n\t"
    "s_add_u32 s70, s70, s79\n\t"
    "s_mul_i32 s64, s70, 6144\n\t"
    "s_bitcmp1_b32 s70, 0\n\t"
    "s_cselect_b32 s78, -6080, 0\n\t"
    "s_add_u32 s64, s64, s78\n\t"
    "s_add_u32 s64, s64, 90112\n\t"
    "s_bfe_u32 s71, s56, 0x30014\n\t"
    "s_add_u32 s71, s71, s79\n\t"
    "s_mul_i32 s65, s71, 6144\n\t"
    "s_bitcmp1_b32 s71, 0\n\t"
    "s_cselect_b32 s78, -6080, 0\n\t"
    "s_add_u32 s65, s65, s78\n\t"
    "s_add_u32 s65, s65, 90112\n\t"
    "s_mul_i32 s72, s36, 6144\n\t"
    "s_mul_i32 s76, s3, 6144\n\t"
    "s_mul_i32 s78, s38, -49024\n\t"
    "s_add_u32 s73, s76, s78\n\t"
    "s_add_u32 s73, s73, 49152\n\t"
    "s_add_u32 s74, s72, 24704\n\t"
    "s_add_u32 s75, s72, 256\n\t"
    "s_add_u32 s76, s76, 49408\n\t"
    "s_mov_b32 s56, 0x05040100\n\t"
    "s_mov_b32 s57, 0x07060302\n\t"
    "v_cmp_gt_u32 vcc, 0x150, v0\n\t"
    "s_waitcnt vmcnt(10)\n\t"
    "s_mov_b64 exec, vcc\n\t"
    "v_perm_b32 v24, v17, v16, s56\n\t"
    "v_perm_b32 v25, v19, v18, s56\n\t"
    "v_lshlrev_b32 v26, 16, v16\n\t"
    "v_perm_b32 v27, v18, v17, s56\n\t"
    "ds_write_b128 v23, v[24:27]\n\t"
    "v_perm_b32 v28, v21, v20, s56\n\t"
    "v_and_b32 v29, s54, v22\n\t"
    "v_perm_b32 v30, v20, v19, s56\n\t"
    "v_perm_b32 v31, v22, v21, s56\n\t"
    "ds_write_b128 v23, v[28:31] offset:800\n\t"
    "v_perm_b32 v32, v17, v16, s57\n\t"
    "v_perm_b32 v33, v19, v18, s57\n\t"
    "v_and_b32 v34, 0xffff0000, v16\n\t"
    "v_perm_b32 v35, v18, v17, s57\n\t"
    "ds_write_b128 v23, v[32:35] offset:1600\n\t"
    "v_perm_b32 v36, v21, v20, s57\n\t"
    "v_lshrrev_b32 v37, 16, v22\n\t"
    "v_perm_b32 v38, v20, v19, s57\n\t"
    "v_perm_b32 v39, v22, v21, s57\n\t"
    "ds_write_b128 v23, v[36:39] offset:2400\n\t"
    "s_mov_b64 exec, -1\n\t"
    "s_waitcnt lgkmcnt(0)\n\t"
    "s_barrier\n\t"
    "ds_read_b64 v[24:25], v72\n\t"
    "ds_read_b64 v[26:27], v72 offset:6400\n\t"
    "ds_read_b64 v[28:29], v72 offset:12800\n\t"
    "ds_read_b64 v[30:31], v72 offset:19200\n\t"
    "ds_read_b64 v[40:41], v73 offset:32\n\t"
    "ds_read_b64 v[42:43], v73 offset:6432\n\t"
    "ds_read_b64 v[44:45], v73 offset:12832\n\t"
    "ds_read_b64 v[46:47], v73 offset:19232\n\t"
    "ds_read_b64 v[56:57], v72 offset:64\n\t"
    "ds_read_b64 v[58:59], v72 offset:6464\n\t"
    "ds_read_b64 v[60:61], v72 offset:12864\n\t"
    "ds_read_b64 v[62:63], v72 offset:19264\n\t"
    "s_waitcnt lgkmcnt(11)\n\t"
    "v_mov_b32_dpp v32, v24 row_ror:8 row_mask:0xf bank_mask:0xf\n\t"
    "v_mov_b32_dpp v33, v25 row_ror:8 row_mask:0xf bank_mask:0xf\n\t"
    "s_waitcnt lgkmcnt(10)\n\t"
    "v_mov_b32_dpp v34, v26 row_ror:8 row_mask:0xf bank_mask:0xf\n\t"
    "v_mov_b32_dpp v35, v27 row_ror:8 row_mask:0xf bank_mask:0xf\n\t"
    "s_waitcnt lgkmcnt(9)\n\t"
    "v_mov_b32_dpp v36, v28 row_ror:8 row_mask:0xf bank_mask:0xf\n\t"
    "v_mov_b32_dpp v37, v29 row_ror:8 row_mask:0xf bank_mask:0xf\n\t"
    "s_waitcnt lgkmcnt(8)\n\t"
    "v_mov_b32_dpp v38, v30 row_ror:8 row_mask:0xf bank_mask:0xf\n\t"
    "v_mov_b32_dpp v39, v31 row_ror:8 row_mask:0xf bank_mask:0xf\n\t"
    "s_waitcnt lgkmcnt(7)\n\t"
    "v_mov_b32_dpp v48, v40 row_ror:8 row_mask:0xf bank_mask:0xf\n\t"
    "v_mov_b32_dpp v49, v41 row_ror:8 row_mask:0xf bank_mask:0xf\n\t"
    "s_waitcnt lgkmcnt(6)\n\t"
    "v_mov_b32_dpp v50, v42 row_ror:8 row_mask:0xf bank_mask:0xf\n\t"
    "v_mov_b32_dpp v51, v43 row_ror:8 row_mask:0xf bank_mask:0xf\n\t"
    "s_waitcnt lgkmcnt(5)\n\t"
    "v_mov_b32_dpp v52, v44 row_ror:8 row_mask:0xf bank_mask:0xf\n\t"
    "v_mov_b32_dpp v53, v45 row_ror:8 row_mask:0xf bank_mask:0xf\n\t"
    "s_waitcnt lgkmcnt(4)\n\t"
    "v_mov_b32_dpp v54, v46 row_ror:8 row_mask:0xf bank_mask:0xf\n\t"
    "v_mov_b32_dpp v55, v47 row_ror:8 row_mask:0xf bank_mask:0xf\n\t"
    "s_waitcnt lgkmcnt(3)\n\t"
    "v_mov_b32_dpp v64, v56 row_ror:8 row_mask:0xf bank_mask:0xf\n\t"
    "v_mov_b32_dpp v65, v57 row_ror:8 row_mask:0xf bank_mask:0xf\n\t"
    "s_waitcnt lgkmcnt(2)\n\t"
    "v_mov_b32_dpp v66, v58 row_ror:8 row_mask:0xf bank_mask:0xf\n\t"
    "v_mov_b32_dpp v67, v59 row_ror:8 row_mask:0xf bank_mask:0xf\n\t"
    "s_waitcnt lgkmcnt(1)\n\t"
    "v_mov_b32_dpp v68, v60 row_ror:8 row_mask:0xf bank_mask:0xf\n\t"
    "v_mov_b32_dpp v69, v61 row_ror:8 row_mask:0xf bank_mask:0xf\n\t"
    "s_waitcnt lgkmcnt(0)\n\t"
    "v_mov_b32_dpp v70, v62 row_ror:8 row_mask:0xf bank_mask:0xf\n\t"
    "v_mov_b32_dpp v71, v63 row_ror:8 row_mask:0xf bank_mask:0xf\n\t"
    "s_waitcnt vmcnt(4) lgkmcnt(0)\n\t"
    "s_barrier\n\t"
    "ds_read_b64_tr_b16 v[108:109], v76\n\t"
    "ds_read_b64_tr_b16 v[110:111], v77 offset:7680\n\t"
    "ds_read_b64_tr_b16 v[112:113], v76 offset:15360\n\t"
    "ds_read_b64_tr_b16 v[114:115], v77 offset:23040\n\t"
    "ds_read_b64_tr_b16 v[128:129], v77 offset:1536\n\t"
    "ds_read_b64_tr_b16 v[130:131], v76 offset:9216\n\t"
    "ds_read_b64_tr_b16 v[132:133], v77 offset:16896\n\t"
    "ds_read_b64_tr_b16 v[134:135], v76 offset:24576\n\t"
    "ds_read_b64_tr_b16 v[148:149], v78\n\t"
    "ds_read_b64_tr_b16 v[150:151], v79 offset:7680\n\t"
    "ds_read_b64_tr_b16 v[152:153], v78 offset:15360\n\t"
    "ds_read_b64_tr_b16 v[154:155], v79 offset:23040\n\t"
    "ds_read_b64_tr_b16 v[188:189], v80\n\t"
    "ds_read_b64_tr_b16 v[190:191], v81 offset:7680\n\t"
    "ds_read_b64_tr_b16 v[192:193], v80 offset:15360\n\t"
    "ds_read_b64_tr_b16 v[194:195], v81 offset:23040\n\t"
    "s_setprio 3\n\t"
    "s_waitcnt lgkmcnt(14)\n\t"
    "v_mfma_f32_16x16x32_bf16 v[0:3], v[24:27], v[108:111], 0\n\t"
    "ds_read_b64_tr_b16 v[168:169], v79 offset:1536\n\t"
    "ds_read_b64_tr_b16 v[170:171], v78 offset:9216\n\t"
    "ds_read_b64_tr_b16 v[172:173], v79 offset:16896\n\t"
    "ds_read_b64_tr_b16 v[174:175], v78 offset:24576\n\t"
    "s_waitcnt lgkmcnt(15)\n\t"
    "v_mfma_f32_16x16x32_bf16 v[0:3], v[28:31], v[112:115], v[0:3]\n\t"
    "s_waitcnt lgkmcnt(14)\n\t"
    "v_mfma_f32_16x16x32_bf16 v[4:7], v[32:35], v[128:131], 0\n\t"
    "s_waitcnt lgkmcnt(12)\n\t"
    "v_mfma_f32_16x16x32_bf16 v[4:7], v[36:39], v[132:135], v[4:7]\n\t"
    "s_mov_b64 exec, -1\n\t"
    "ds_read_b64_tr_b16 v[116:117], v76 offset:30720\n\t"
    "ds_read_b64_tr_b16 v[136:137], v77 offset:32256\n\t"
    "s_add_u32 m0, s32, 90112\n\t"
    "s_add_u32 s78, s72, 0\n\t"
    "buffer_load_dwordx4 v82, s[8:11], s78 offen lds\n\t"
    "s_waitcnt lgkmcnt(12)\n\t"
    "v_mfma_f32_16x16x32_bf16 v[8:11], v[40:43], v[148:151], 0\n\t"
    "s_mov_b64 exec, s[44:45]\n\t"
    "v_cvt_pk_bf16_f32 v228, v0, v1\n\t"
    "v_cvt_pk_bf16_f32 v229, v2, v3\n\t"
    "s_mov_b64 exec, -1\n\t"
    "ds_read_b64_tr_b16 v[208:209], v81 offset:1536\n\t"
    "ds_read_b64_tr_b16 v[210:211], v80 offset:9216\n\t"
    "ds_read_b64_tr_b16 v[212:213], v81 offset:16896\n\t"
    "ds_read_b64_tr_b16 v[214:215], v80 offset:24576\n\t"
    "s_waitcnt lgkmcnt(14)\n\t"
    "v_mfma_f32_16x16x32_bf16 v[8:11], v[44:47], v[152:155], v[8:11]\n\t"
    "s_mov_b64 exec, s[46:47]\n\t"
    "v_cvt_pk_bf16_f32 v228, v4, v5\n\t"
    "v_cvt_pk_bf16_f32 v229, v6, v7\n\t"
    "s_waitcnt lgkmcnt(8)\n\t"
    "v_mfma_f32_16x16x32_bf16 v[12:15], v[48:51], v[168:171], 0\n\t"
    "s_waitcnt lgkmcnt(6)\n\t"
    "v_mfma_f32_16x16x32_bf16 v[12:15], v[52:55], v[172:175], v[12:15]\n\t"
    "s_mov_b64 exec, -1\n\t"
    "ds_read_b64_tr_b16 v[156:157], v78 offset:30720\n\t"
    "ds_read_b64_tr_b16 v[176:177], v79 offset:32256\n\t"
    "s_add_u32 m0, s32, 98304\n\t"
    "s_add_u32 s78, s73, 0\n\t"
    "buffer_load_dwordx4 v82, s[8:11], s78 offen lds\n\t"
    "v_mfma_f32_16x16x32_bf16 v[16:19], v[56:59], v[188:191], 0\n\t"
    "s_mov_b64 exec, s[80:81]\n\t"
    "v_cvt_pk_bf16_f32 v228, v8, v9\n\t"
    "v_cvt_pk_bf16_f32 v229, v10, v11\n\t"
    "s_mov_b64 exec, -1\n\t"
    "v_mfma_f32_16x16x32_bf16 v[16:19], v[60:63], v[192:195], v[16:19]\n\t"
    "s_mov_b64 exec, s[82:83]\n\t"
    "v_cvt_pk_bf16_f32 v228, v12, v13\n\t"
    "v_cvt_pk_bf16_f32 v229, v14, v15\n\t"
    "s_waitcnt lgkmcnt(4)\n\t"
    "v_mfma_f32_16x16x32_bf16 v[20:23], v[64:67], v[208:211], 0\n\t"
    "s_waitcnt lgkmcnt(2)\n\t"
    "v_mfma_f32_16x16x32_bf16 v[20:23], v[68:71], v[212:215], v[20:23]\n\t"
    "s_mov_b64 exec, -1\n\t"
    "ds_read_b64_tr_b16 v[196:197], v80 offset:30720\n\t"
    "ds_read_b64_tr_b16 v[216:217], v81 offset:32256\n\t"
    "s_add_u32 m0, s32, 106496\n\t"
    "s_add_u32 s78, s74, 0\n\t"
    "buffer_load_dwordx4 v82, s[8:11], s78 offen lds\n\t"
    "s_waitcnt lgkmcnt(0)\n\t"
    "s_setprio 2\n\t"
    "v_mfma_f32_16x16x32_bf16 v[0:3], v[24:27], v[110:113], 0\n\t"
    "s_mov_b64 exec, s[44:45]\n\t"
    "v_cvt_pk_bf16_f32 v230, v16, v17\n\t"
    "v_cvt_pk_bf16_f32 v231, v18, v19\n\t"
    "v_mfma_f32_16x16x32_bf16 v[0:3], v[28:31], v[114:117], v[0:3]\n\t"
    "s_mov_b64 exec, s[46:47]\n\t"
    "v_cvt_pk_bf16_f32 v230, v20, v21\n\t"
    "v_cvt_pk_bf16_f32 v231, v22, v23\n\t"
    "v_mfma_f32_16x16x32_bf16 v[4:7], v[32:35], v[130:133], 0\n\t"
    "v_mfma_f32_16x16x32_bf16 v[4:7], v[36:39], v[134:137], v[4:7]\n\t"
    "s_mov_b64 exec, -1\n\t"
    "ds_read_b64_tr_b16 v[118:119], v77 offset:38400\n\t"
    "ds_read_b64_tr_b16 v[138:139], v76 offset:39936\n\t"
    "s_add_u32 m0, s32, 114688\n\t"
    "s_add_u32 s78, s75, 0\n\t"
    "buffer_load_dwordx4 v82, s[8:11], s78 offen lds\n\t"
    "v_mfma_f32_16x16x32_bf16 v[8:11], v[40:43], v[150:153], 0\n\t"
    "s_mov_b64 exec, s[44:45]\n\t"
    "v_cvt_pk_bf16_f32 v232, v0, v1\n\t"
    "v_cvt_pk_bf16_f32 v233, v2, v3\n\t"
    "v_mfma_f32_16x16x32_bf16 v[8:11], v[44:47], v[154:157], v[8:11]\n\t"
    "s_mov_b64 exec, s[46:47]\n\t"
    "v_cvt_pk_bf16_f32 v232, v4, v5\n\t"
    "v_cvt_pk_bf16_f32 v233, v6, v7\n\t"
    "v_mfma_f32_16x16x32_bf16 v[12:15], v[48:51], v[170:173], 0\n\t"
    "v_mfma_f32_16x16x32_bf16 v[12:15], v[52:55], v[174:177], v[12:15]\n\t"
    "s_mov_b64 exec, -1\n\t"
    "ds_read_b64_tr_b16 v[158:159], v79 offset:38400\n\t"
    "ds_read_b64_tr_b16 v[178:179], v78 offset:39936\n\t"
    "s_add_u32 m0, s33, 122880\n\t"
    "s_add_u32 s78, s76, 0\n\t"
    "buffer_load_dwordx4 v82, s[8:11], s78 offen lds\n\t"
    "s_setprio 1\n\t"
    "v_mfma_f32_16x16x32_bf16 v[16:19], v[56:59], v[190:193], 0\n\t"
    "s_mov_b64 exec, s[80:81]\n\t"
    "v_cvt_pk_bf16_f32 v232, v8, v9\n\t"
    "v_cvt_pk_bf16_f32 v233, v10, v11\n\t"
    "v_mfma_f32_16x16x32_bf16 v[16:19], v[60:63], v[194:197], v[16:19]\n\t"
    "s_mov_b64 exec, s[82:83]\n\t"
    "v_cvt_pk_bf16_f32 v232, v12, v13\n\t"
    "v_cvt_pk_bf16_f32 v233, v14, v15\n\t"
    "v_mfma_f32_16x16x32_bf16 v[20:23], v[64:67], v[210:213], 0\n\t"
    "v_mfma_f32_16x16x32_bf16 v[20:23], v[68:71], v[214:217], v[20:23]\n\t"
    "s_mov_b64 exec, -1\n\t"
    "ds_read_b64_tr_b16 v[198:199], v81 offset:38400\n\t"
    "ds_read_b64_tr_b16 v[218:219], v80 offset:39936\n\t"
    "s_waitcnt lgkmcnt(0)\n\t"
    "v_mfma_f32_16x16x32_bf16 v[0:3], v[24:27], v[112:115], 0\n\t"
    "s_mov_b64 exec, s[44:45]\n\t"
    "v_cvt_pk_bf16_f32 v234, v16, v17\n\t"
    "v_cvt_pk_bf16_f32 v235, v18, v19\n\t"
    "v_mfma_f32_16x16x32_bf16 v[0:3], v[28:31], v[116:119], v[0:3]\n\t"
    "s_mov_b64 exec, s[46:47]\n\t"
    "v_cvt_pk_bf16_f32 v234, v20, v21\n\t"
    "v_cvt_pk_bf16_f32 v235, v22, v23\n\t"
    "v_mfma_f32_16x16x32_bf16 v[4:7], v[32:35], v[132:135], 0\n\t"
    "v_mfma_f32_16x16x32_bf16 v[4:7], v[36:39], v[136:139], v[4:7]\n\t"
    "s_setprio 0\n\t"
    "v_mfma_f32_16x16x32_bf16 v[8:11], v[40:43], v[152:155], 0\n\t"
    "s_mov_b64 exec, s[44:45]\n\t"
    "v_cvt_pk_bf16_f32 v236, v0, v1\n\t"
    "v_cvt_pk_bf16_f32 v237, v2, v3\n\t"
    "v_mfma_f32_16x16x32_bf16 v[8:11], v[44:47], v[156:159], v[8:11]\n\t"
    "s_mov_b64 exec, s[46:47]\n\t"
    "s_nop 0\n\t"
    "v_cvt_pk_bf16_f32 v236, v4, v5\n\t"
    "v_cvt_pk_bf16_f32 v237, v6, v7\n\t"
    "v_mfma_f32_16x16x32_bf16 v[12:15], v[48:51], v[172:175], 0\n\t"
    "v_mfma_f32_16x16x32_bf16 v[12:15], v[52:55], v[176:179], v[12:15]\n\t"
    "v_mfma_f32_16x16x32_bf16 v[16:19], v[56:59], v[192:195], 0\n\t"
    "s_mov_b64 exec, s[80:81]\n\t"
    "v_cvt_pk_bf16_f32 v236, v8, v9\n\t"
    "v_cvt_pk_bf16_f32 v237, v10, v11\n\t"
    "v_mfma_f32_16x16x32_bf16 v[16:19], v[60:63], v[196:199], v[16:19]\n\t"
    "s_mov_b64 exec, s[82:83]\n\t"
    "s_nop 1\n\t"
    "v_cvt_pk_bf16_f32 v236, v12, v13\n\t"
    "v_cvt_pk_bf16_f32 v237, v14, v15\n\t"
    "v_mfma_f32_16x16x32_bf16 v[20:23], v[64:67], v[212:215], 0\n\t"
    "v_mfma_f32_16x16x32_bf16 v[20:23], v[68:71], v[216:219], v[20:23]\n\t"
    "s_waitcnt vmcnt(7) lgkmcnt(0)\n\t"
    "s_barrier\n\t"
    "s_mov_b64 exec, -1\n\t"
    "ds_read_b64_tr_b16 v[120:121], v76 offset:46080\n\t"
    "ds_read_b64_tr_b16 v[140:141], v77 offset:47616\n\t"
    "ds_read_b64_tr_b16 v[160:161], v78 offset:46080\n\t"
    "ds_read_b64_tr_b16 v[180:181], v79 offset:47616\n\t"
    "ds_read_b64_tr_b16 v[200:201], v80 offset:46080\n\t"
    "ds_read_b64_tr_b16 v[220:221], v81 offset:47616\n\t"
    "s_setprio 3\n\t"
    "v_mfma_f32_16x16x32_bf16 v[0:3], v[24:27], v[114:117], 0\n\t"
    "s_mov_b64 exec, s[44:45]\n\t"
    "v_cvt_pk_bf16_f32 v238, v16, v17\n\t"
    "v_cvt_pk_bf16_f32 v239, v18, v19\n\t"
    "s_waitcnt lgkmcnt(5)\n\t"
    "v_mfma_f32_16x16x32_bf16 v[0:3], v[28:31], v[118:121], v[0:3]\n\t"
    "s_mov_b64 exec, s[46:47]\n\t"
    "v_cvt_pk_bf16_f32 v238, v20, v21\n\t"
    "v_cvt_pk_bf16_f32 v239, v22, v23\n\t"
    "v_mfma_f32_16x16x32_bf16 v[4:7], v[32:35], v[134:137], 0\n\t"
    "s_mov_b64 exec, s[48:49]\n\t"
    "ds_write_b64 v75, v[238:239] offset:3440\n\t"
    "s_waitcnt lgkmcnt(5)\n\t"
    "v_mfma_f32_16x16x32_bf16 v[4:7], v[36:39], v[138:141], v[4:7]\n\t"
    "s_mov_b64 exec, -1\n\t"
    "ds_read_b64_tr_b16 v[122:123], v77 offset:53760\n\t"
    "ds_read_b64_tr_b16 v[142:143], v76 offset:55296\n\t"
    "s_add_u32 m0, s32, 126976\n\t"
    "s_add_u32 s78, s72, 384\n\t"
    "buffer_load_dwordx4 v82, s[8:11], s78 offen lds\n\t"
    "v_mfma_f32_16x16x32_bf16 v[8:11], v[40:43], v[154:157], 0\n\t"
    "s_mov_b64 exec, s[44:45]\n\t"
    "v_cvt_pk_bf16_f32 v240, v0, v1\n\t"
    "v_cvt_pk_bf16_f32 v241, v2, v3\n\t"
    "s_waitcnt lgkmcnt(6)\n\t"
    "v_mfma_f32_16x16x32_bf16 v[8:11], v[44:47], v[158:161], v[8:11]\n\t"
    "s_mov_b64 exec, s[46:47]\n\t"
    "v_cvt_pk_bf16_f32 v240, v4, v5\n\t"
    "v_cvt_pk_bf16_f32 v241, v6, v7\n\t"
    "v_mfma_f32_16x16x32_bf16 v[12:15], v[48:51], v[174:177], 0\n\t"
    "s_mov_b64 exec, s[84:85]\n\t"
    "ds_write_b64 v74, v[236:237] offset:112\n\t"
    "s_mov_b64 exec, s[48:49]\n\t"
    "ds_write_b64 v75, v[234:235] offset:3384\n\t"
    "s_mov_b64 exec, s[84:85]\n\t"
    "ds_write_b64 v74, v[232:233] offset:56\n\t"
    "s_waitcnt lgkmcnt(8)\n\t"
    "v_mfma_f32_16x16x32_bf16 v[12:15], v[52:55], v[178:181], v[12:15]\n\t"
    "s_mov_b64 exec, -1\n\t"
    "ds_read_b64_tr_b16 v[162:163], v79 offset:53760\n\t"
    "ds_read_b64_tr_b16 v[182:183], v78 offset:55296\n\t"
    "s_add_u32 m0, s32, 135168\n\t"
    "s_add_u32 s78, s73, 384\n\t"
    "buffer_load_dwordx4 v82, s[8:11], s78 offen lds\n\t"
    "s_setprio 2\n\t"
    "v_mfma_f32_16x16x32_bf16 v[16:19], v[56:59], v[194:197], 0\n\t"
    "s_mov_b64 exec, s[80:81]\n\t"
    "v_cvt_pk_bf16_f32 v240, v8, v9\n\t"
    "v_cvt_pk_bf16_f32 v241, v10, v11\n\t"
    "s_waitcnt lgkmcnt(9)\n\t"
    "v_mfma_f32_16x16x32_bf16 v[16:19], v[60:63], v[198:201], v[16:19]\n\t"
    "s_mov_b64 exec, s[82:83]\n\t"
    "v_cvt_pk_bf16_f32 v240, v12, v13\n\t"
    "v_cvt_pk_bf16_f32 v241, v14, v15\n\t"
    "v_mfma_f32_16x16x32_bf16 v[20:23], v[64:67], v[214:217], 0\n\t"
    "s_mov_b64 exec, s[84:85]\n\t"
    "ds_write_b64 v74, v[240:241] offset:168\n\t"
    "s_waitcnt lgkmcnt(9)\n\t"
    "v_mfma_f32_16x16x32_bf16 v[20:23], v[68:71], v[218:221], v[20:23]\n\t"
    "s_mov_b64 exec, -1\n\t"
    "ds_read_b64_tr_b16 v[202:203], v81 offset:53760\n\t"
    "ds_read_b64_tr_b16 v[222:223], v80 offset:55296\n\t"
    "s_add_u32 m0, s32, 143360\n\t"
    "s_add_u32 s78, s74, 384\n\t"
    "buffer_load_dwordx4 v82, s[8:11], s78 offen lds\n\t"
    "s_waitcnt lgkmcnt(0)\n\t"
    "v_mfma_f32_16x16x32_bf16 v[0:3], v[24:27], v[116:119], 0\n\t"
    "s_mov_b64 exec, s[44:45]\n\t"
    "v_cvt_pk_bf16_f32 v242, v16, v17\n\t"
    "v_cvt_pk_bf16_f32 v243, v18, v19\n\t"
    "v_mfma_f32_16x16x32_bf16 v[0:3], v[28:31], v[120:123], v[0:3]\n\t"
    "s_mov_b64 exec, s[46:47]\n\t"
    "v_cvt_pk_bf16_f32 v242, v20, v21\n\t"
    "v_cvt_pk_bf16_f32 v243, v22, v23\n\t"
    "v_mfma_f32_16x16x32_bf16 v[4:7], v[32:35], v[136:139], 0\n\t"
    "s_mov_b64 exec, s[48:49]\n\t"
    "ds_write_b64 v75, v[242:243] offset:3496\n\t"
    "v_mfma_f32_16x16x32_bf16 v[4:7], v[36:39], v[140:143], v[4:7]\n\t"
    "s_mov_b64 exec, -1\n\t"
    "s_add_u32 m0, s32, 151552\n\t"
    "s_add_u32 s78, s75, 384\n\t"
    "buffer_load_dwordx4 v82, s[8:11], s78 offen lds\n\t"
    "s_setprio 1\n\t"
    "v_mfma_f32_16x16x32_bf16 v[8:11], v[40:43], v[156:159], 0\n\t"
    "s_mov_b64 exec, s[44:45]\n\t"
    "v_cvt_pk_bf16_f32 v240, v0, v1\n\t"
    "v_cvt_pk_bf16_f32 v241, v2, v3\n\t"
    "v_mfma_f32_16x16x32_bf16 v[8:11], v[44:47], v[160:163], v[8:11]\n\t"
    "s_mov_b64 exec, s[46:47]\n\t"
    "v_cvt_pk_bf16_f32 v240, v4, v5\n\t"
    "v_cvt_pk_bf16_f32 v241, v6, v7\n\t"
    "v_mfma_f32_16x16x32_bf16 v[12:15], v[48:51], v[176:179], 0\n\t"
    "s_mov_b64 exec, s[48:49]\n\t"
    "ds_write_b64 v75, v[230:231] offset:3328\n\t"
    "s_mov_b64 exec, s[84:85]\n\t"
    "ds_write_b64 v74, v[228:229]\n\t"
    "v_mfma_f32_16x16x32_bf16 v[12:15], v[52:55], v[180:183], v[12:15]\n\t"
    "s_mov_b64 exec, -1\n\t"
    "s_add_u32 m0, s33, 159744\n\t"
    "s_add_u32 s78, s76, 384\n\t"
    "buffer_load_dwordx4 v82, s[8:11], s78 offen lds\n\t"
    "s_setprio 0\n\t"
    "v_mfma_f32_16x16x32_bf16 v[16:19], v[56:59], v[196:199], 0\n\t"
    "s_mov_b64 exec, s[80:81]\n\t"
    "v_cvt_pk_bf16_f32 v240, v8, v9\n\t"
    "v_cvt_pk_bf16_f32 v241, v10, v11\n\t"
    "v_mfma_f32_16x16x32_bf16 v[16:19], v[60:63], v[200:203], v[16:19]\n\t"
    "s_mov_b64 exec, s[82:83]\n\t"
    "v_cvt_pk_bf16_f32 v240, v12, v13\n\t"
    "v_cvt_pk_bf16_f32 v241, v14, v15\n\t"
    "v_mfma_f32_16x16x32_bf16 v[20:23], v[64:67], v[216:219], 0\n\t"
    "s_mov_b64 exec, s[84:85]\n\t"
    "ds_write_b64 v74, v[240:241] offset:224\n\t"
    "v_mfma_f32_16x16x32_bf16 v[20:23], v[68:71], v[220:223], v[20:23]\n\t"
    "s_mov_b64 exec, s[44:45]\n\t"
    "v_cvt_pk_bf16_f32 v242, v16, v17\n\t"
    "v_cvt_pk_bf16_f32 v243, v18, v19\n\t"
    "s_mov_b64 exec, s[46:47]\n\t"
    "s_nop 3\n\t"
    "v_cvt_pk_bf16_f32 v242, v20, v21\n\t"
    "v_cvt_pk_bf16_f32 v243, v22, v23\n\t"
    "s_mov_b64 exec, s[48:49]\n\t"
    "ds_write_b64 v75, v[242:243] offset:3552\n\t"
    "s_waitcnt vmcnt(10) lgkmcnt(0)\n\t"
    "s_barrier\n\t"
    "s_mov_b64 exec, -1\n\t"
    "buffer_load_dwordx2 v[250:251], off, s[16:19], 0 sc1\n\t"
    "v_add_u32 v76, 15360, v76\n\t"
    "v_add_u32 v77, 15360, v77\n\t"
    "v_add_u32 v78, 15360, v78\n\t"
    "v_add_u32 v79, 15360, v79\n\t"
    "v_add_u32 v80, 15360, v80\n\t"
    "v_add_u32 v81, 15360, v81\n\t"
    "ds_read_b64_tr_b16 v[124:125], v76 offset:46080\n\t"
    "ds_read_b64_tr_b16 v[144:145], v77 offset:47616\n\t"
    "ds_read_b64_tr_b16 v[164:165], v78 offset:46080\n\t"
    "ds_read_b64_tr_b16 v[184:185], v79 offset:47616\n\t"
    "ds_read_b64_tr_b16 v[204:205], v80 offset:46080\n\t"
    "ds_read_b64_tr_b16 v[224:225], v81 offset:47616\n\t"
    "s_setprio 3\n\t"
    "v_mfma_f32_16x16x32_bf16 v[0:3], v[24:27], v[118:121], 0\n\t"
    "s_waitcnt lgkmcnt(5)\n\t"
    "v_mfma_f32_16x16x32_bf16 v[0:3], v[28:31], v[122:125], v[0:3]\n\t"
    "v_mfma_f32_16x16x32_bf16 v[4:7], v[32:35], v[138:141], 0\n\t"
    "s_mov_b64 exec, -1\n\t"
    "v_add_u32 v117, s34, v83\n\t"
    "ds_read_b64_tr_b16 v[108:109], v117\n\t"
    "ds_read_b64_tr_b16 v[110:111], v117 offset:6656\n\t"
    "ds_read_b64_tr_b16 v[112:113], v117 offset:13312\n\t"
    "ds_read_b64_tr_b16 v[114:115], v117 offset:19968\n\t"
    "ds_read_b64_tr_b16 v[128:129], v117 offset:26624\n\t"
    "ds_read_b64_tr_b16 v[130:131], v117 offset:33280\n\t"
    "v_mov_b32 v152, 0x3f803f80\n\t"
    "v_mov_b32 v153, 0x3f803f80\n\t"
    "v_mov_b32 v154, 0x3f803f80\n\t"
    "v_mov_b32 v155, 0x3f803f80\n\t"
    "v_bfe_u32 v168, v86, 0, 1\n\t"
    "s_waitcnt lgkmcnt(10)\n\t"
    "v_mfma_f32_16x16x32_bf16 v[4:7], v[36:39], v[142:145], v[4:7]\n\t"
    "ds_read_b64_tr_b16 v[126:127], v77 offset:53760\n\t"
    "ds_read_b64_tr_b16 v[146:147], v76 offset:55296\n\t"
    "v_cvt_f32_u32 v168, v168\n\t"
    "v_bfe_u32 v169, v86, 1, 1\n\t"
    "v_cvt_f32_u32 v169, v169\n\t"
    "v_bfe_u32 v170, v86, 2, 1\n\t"
    "v_cvt_f32_u32 v170, v170\n\t"
    "v_bfe_u32 v171, v86, 3, 1\n\t"
    "v_cvt_f32_u32 v171, v171\n\t"
    "s_waitcnt lgkmcnt(6)\n\t"
    "v_mfma_f32_16x16x32_bf16 v[148:151], v[108:111], v[108:111], 0\n\t"
    "v_mfma_f32_16x16x32_bf16 v[132:135], v[152:155], v[108:111], 0\n\t"
    "v_mfma_f32_16x16x32_bf16 v[8:11], v[40:43], v[158:161], 0\n\t"
    "s_mov_b64 exec, s[44:45]\n\t"
    "v_cvt_pk_bf16_f32 v244, v0, v1\n\t"
    "v_cvt_pk_bf16_f32 v245, v2, v3\n\t"
    "v_mfma_f32_16x16x32_bf16 v[8:11], v[44:47], v[162:165], v[8:11]\n\t"
    "s_mov_b64 exec, s[46:47]\n\t"
    "v_cvt_pk_bf16_f32 v244, v4, v5\n\t"
    "v_cvt_pk_bf16_f32 v245, v6, v7\n\t"
    "v_mfma_f32_16x16x32_bf16 v[12:15], v[48:51], v[178:181], 0\n\t"
    "s_mov_b64 exec, -1\n\t"
    "s_waitcnt lgkmcnt(4)\n\t"
    "v_mfma_f32_16x16x32_bf16 v[148:151], v[112:115], v[112:115], v[148:151]\n\t"
    "v_mfma_f32_16x16x32_bf16 v[132:135], v[152:155], v[112:115], v[132:135]\n\t"
    "s_waitcnt lgkmcnt(2)\n\t"
    "v_mfma_f32_16x16x32_bf16 v[148:151], v[128:131], v[128:131], v[148:151]\n\t"
    "v_mfma_f32_16x16x32_bf16 v[132:135], v[152:155], v[128:131], v[132:135]\n\t"
    "v_mov_b32 v116, 0x383ef424\n\t"
    "v_add_u32 v136, s34, v84\n\t"
    "v_mfma_f32_16x16x32_bf16 v[12:15], v[52:55], v[182:185], v[12:15]\n\t"
    "ds_read_b64_tr_b16 v[166:167], v79 offset:53760\n\t"
    "ds_read_b64_tr_b16 v[186:187], v78 offset:55296\n\t"
    "s_nop 1\n\t"
    "v_mul_f32 v148, v148, v168\n\t"
    "v_fmac_f32 v148, v149, v169\n\t"
    "v_fmac_f32 v148, v150, v170\n\t"
    "v_fmac_f32 v148, v151, v171\n\t"
    "v_mul_f32 v132, 0x3c2aaaab, v132\n\t"
    "v_mov_b32 v149, v148\n\t"
    "v_mul_f32 v133, v132, v132\n\t"
    "v_lshlrev_b32 v172, 16, v108\n\t"
    "v_permlane16_swap_b32 v148, v149\n\t"
    "s_setprio 2\n\t"
    "v_mfma_f32_16x16x32_bf16 v[16:19], v[56:59], v[198:201], 0\n\t"
    "s_mov_b64 exec, s[80:81]\n\t"
    "v_cvt_pk_bf16_f32 v244, v8, v9\n\t"
    "v_cvt_pk_bf16_f32 v245, v10, v11\n\t"
    "v_mfma_f32_16x16x32_bf16 v[16:19], v[60:63], v[202:205], v[16:19]\n\t"
    "s_mov_b64 exec, s[82:83]\n\t"
    "v_cvt_pk_bf16_f32 v244, v12, v13\n\t"
    "v_cvt_pk_bf16_f32 v245, v14, v15\n\t"
    "s_mov_b64 exec, s[84:85]\n\t"
    "ds_write_b64 v74, v[244:245] offset:280\n\t"
    "v_mfma_f32_16x16x32_bf16 v[20:23], v[64:67], v[218:221], 0\n\t"
    "s_mov_b64 exec, -1\n\t"
    "v_add_f32 v148, v148, v149\n\t"
    "v_mov_b32 v149, v148\n\t"
    "v_and_b32 v173, 0xffff0000, v108\n\t"
    "v_lshlrev_b32 v174, 16, v109\n\t"
    "v_permlane32_swap_b32 v148, v149\n\t"
    "v_add_f32 v148, v148, v149\n\t"
    "v_fma_f32 v148, v148, s52, -v133\n\t"
    "v_max_f32 v148, 0, v148\n\t"
    "v_fmamk_f32 v148, v148, 0x4091afa4, v116\n\t"
    "v_mfma_f32_16x16x32_bf16 v[20:23], v[68:71], v[222:225], v[20:23]\n\t"
    "ds_read_b64_tr_b16 v[206:207], v81 offset:53760\n\t"
    "ds_read_b64_tr_b16 v[226:227], v80 offset:55296\n\t"
    "v_rsq_f32 v148, v148\n\t"
    "v_and_b32 v175, 0xffff0000, v109\n\t"
    "v_mul_f32_e64 v132, -v132, v148\n\t"
    "v_fma_f32 v172, v172, v148, v132\n\t"
    "v_fma_f32 v173, v173, v148, v132\n\t"
    "v_cvt_pk_bf16_f32 v88, v172, v173\n\t"
    "v_fma_f32 v174, v174, v148, v132\n\t"
    "v_fma_f32 v175, v175, v148, v132\n\t"
    "v_cvt_pk_bf16_f32 v89, v174, v175\n\t"
    "s_waitcnt lgkmcnt(0)\n\t"
    "s_setprio 1\n\t"
    "v_mfma_f32_16x16x32_bf16 v[0:3], v[24:27], v[120:123], 0\n\t"
    "s_mov_b64 exec, s[44:45]\n\t"
    "v_cvt_pk_bf16_f32 v246, v16, v17\n\t"
    "v_cvt_pk_bf16_f32 v247, v18, v19\n\t"
    "v_mfma_f32_16x16x32_bf16 v[0:3], v[28:31], v[124:127], v[0:3]\n\t"
    "s_mov_b64 exec, s[46:47]\n\t"
    "v_cvt_pk_bf16_f32 v246, v20, v21\n\t"
    "v_cvt_pk_bf16_f32 v247, v22, v23\n\t"
    "s_mov_b64 exec, s[48:49]\n\t"
    "ds_write_b64 v75, v[246:247] offset:3608\n\t"
    "v_mfma_f32_16x16x32_bf16 v[4:7], v[32:35], v[140:143], 0\n\t"
    "s_mov_b64 exec, -1\n\t"
    "v_lshlrev_b32 v172, 16, v110\n\t"
    "v_and_b32 v173, 0xffff0000, v110\n\t"
    "v_fma_f32 v172, v172, v148, v132\n\t"
    "v_fma_f32 v173, v173, v148, v132\n\t"
    "v_cvt_pk_bf16_f32 v90, v172, v173\n\t"
    "v_lshlrev_b32 v174, 16, v111\n\t"
    "v_and_b32 v175, 0xffff0000, v111\n\t"
    "v_fma_f32 v174, v174, v148, v132\n\t"
    "v_fma_f32 v175, v175, v148, v132\n\t"
    "v_mfma_f32_16x16x32_bf16 v[4:7], v[36:39], v[144:147], v[4:7]\n\t"
    "v_cvt_pk_bf16_f32 v91, v174, v175\n\t"
    "ds_write_b128 v136, v[88:91]\n\t"
    "v_lshlrev_b32 v172, 16, v112\n\t"
    "v_and_b32 v173, 0xffff0000, v112\n\t"
    "v_fma_f32 v172, v172, v148, v132\n\t"
    "v_fma_f32 v173, v173, v148, v132\n\t"
    "v_cvt_pk_bf16_f32 v92, v172, v173\n\t"
    "v_lshlrev_b32 v174, 16, v113\n\t"
    "v_and_b32 v175, 0xffff0000, v113\n\t"
    "v_mfma_f32_16x16x32_bf16 v[8:11], v[40:43], v[160:163], 0\n\t"
    "s_mov_b64 exec, s[44:45]\n\t"
    "v_cvt_pk_bf16_f32 v240, v0, v1\n\t"
    "v_cvt_pk_bf16_f32 v241, v2, v3\n\t"
    "v_mfma_f32_16x16x32_bf16 v[8:11], v[44:47], v[164:167], v[8:11]\n\t"
    "s_mov_b64 exec, s[46:47]\n\t"
    "v_cvt_pk_bf16_f32 v240, v4, v5\n\t"
    "v_cvt_pk_bf16_f32 v241, v6, v7\n\t"
    "v_mfma_f32_16x16x32_bf16 v[12:15], v[48:51], v[180:183], 0\n\t"
    "s_mov_b64 exec, -1\n\t"
    "v_fma_f32 v174, v174, v148, v132\n\t"
    "v_fma_f32 v175, v175, v148, v132\n\t"
    "v_cvt_pk_bf16_f32 v93, v174, v175\n\t"
    "v_lshlrev_b32 v172, 16, v114\n\t"
    "v_and_b32 v173, 0xffff0000, v114\n\t"
    "v_fma_f32 v172, v172, v148, v132\n\t"
    "v_fma_f32 v173, v173, v148, v132\n\t"
    "v_cvt_pk_bf16_f32 v94, v172, v173\n\t"
    "v_lshlrev_b32 v174, 16, v115\n\t"
    "v_mfma_f32_16x16x32_bf16 v[12:15], v[52:55], v[184:187], v[12:15]\n\t"
    "v_and_b32 v175, 0xffff0000, v115\n\t"
    "v_fma_f32 v174, v174, v148, v132\n\t"
    "v_fma_f32 v175, v175, v148, v132\n\t"
    "v_cvt_pk_bf16_f32 v95, v174, v175\n\t"
    "ds_write_b128 v136, v[92:95] offset:13312\n\t"
    "v_lshlrev_b32 v172, 16, v128\n\t"
    "v_and_b32 v173, 0xffff0000, v128\n\t"
    "v_fma_f32 v172, v172, v148, v132\n\t"
    "v_fma_f32 v173, v173, v148, v132\n\t"
    "s_setprio 0\n\t"
    "v_mfma_f32_16x16x32_bf16 v[16:19], v[56:59], v[200:203], 0\n\t"
    "s_mov_b64 exec, s[80:81]\n\t"
    "v_cvt_pk_bf16_f32 v240, v8, v9\n\t"
    "v_cvt_pk_bf16_f32 v241, v10, v11\n\t"
    "v_mfma_f32_16x16x32_bf16 v[16:19], v[60:63], v[204:207], v[16:19]\n\t"
    "s_mov_b64 exec, s[82:83]\n\t"
    "v_cvt_pk_bf16_f32 v240, v12, v13\n\t"
    "v_cvt_pk_bf16_f32 v241, v14, v15\n\t"
    "s_mov_b64 exec, s[84:85]\n\t"
    "ds_write_b64 v74, v[240:241] offset:336\n\t"
    "v_mfma_f32_16x16x32_bf16 v[20:23], v[64:67], v[220:223], 0\n\t"
    "s_mov_b64 exec, -1\n\t"
    "v_cvt_pk_bf16_f32 v96, v172, v173\n\t"
    "v_lshlrev_b32 v174, 16, v129\n\t"
    "v_and_b32 v175, 0xffff0000, v129\n\t"
    "v_fma_f32 v174, v174, v148, v132\n\t"
    "v_fma_f32 v175, v175, v148, v132\n\t"
    "v_cvt_pk_bf16_f32 v97, v174, v175\n\t"
    "v_lshlrev_b32 v172, 16, v130\n\t"
    "v_and_b32 v173, 0xffff0000, v130\n\t"
    "v_fma_f32 v172, v172, v148, v132\n\t"
    "v_mfma_f32_16x16x32_bf16 v[20:23], v[68:71], v[224:227], v[20:23]\n\t"
    "v_fma_f32 v173, v173, v148, v132\n\t"
    "v_cvt_pk_bf16_f32 v98, v172, v173\n\t"
    "v_lshlrev_b32 v174, 16, v131\n\t"
    "v_and_b32 v175, 0xffff0000, v131\n\t"
    "v_fma_f32 v174, v174, v148, v132\n\t"
    "v_fma_f32 v175, v175, v148, v132\n\t"
    "v_cvt_pk_bf16_f32 v99, v174, v175\n\t"
    "ds_write_b128 v136, v[96:99] offset:26624\n\t"
    "s_mov_b64 exec, s[44:45]\n\t"
    "v_cvt_pk_bf16_f32 v242, v16, v17\n\t"
    "v_cvt_pk_bf16_f32 v243, v18, v19\n\t"
    "s_mov_b64 exec, s[46:47]\n\t"
    "v_cvt_pk_bf16_f32 v242, v20, v21\n\t"
    "v_cvt_pk_bf16_f32 v243, v22, v23\n\t"
    "s_mov_b64 exec, s[48:49]\n\t"
    "ds_write_b64 v75, v[242:243] offset:3664\n\t"
    "s_mov_b64 exec, -1\n\t"
    "s_waitcnt vmcnt(0) lgkmcnt(0)\n\t"
    "s_barrier\n\t"
    "s_xor_b32 s78, s34, 128\n\t"
    "v_add_u32 v60, s78, v84\n\t"
    "v_xor_b32 v102, s60, v100\n\t"
    "v_xor_b32 v103, s60, v101\n\t"
    "s_cmp_gt_u32 s34, 128\n\t"
    "s_cbranch_scc1 7f\n\t"
    "s_cmp_eq_u32 s34, 128\n\t"
    "s_cbranch_scc1 8f\n\t"
    "v_add_u32 v61, s34, v83\n\t"
    "v_add_u32 v61, 256, v61\n\t"
    "ds_read_b64_tr_b16 v[176:177], v61\n\t"
    "ds_read_b64_tr_b16 v[178:179], v61 offset:6656\n\t"
    "ds_read_b64_tr_b16 v[180:181], v61 offset:13312\n\t"
    "ds_read_b64_tr_b16 v[182:183], v61 offset:19968\n\t"
    "ds_read_b64_tr_b16 v[184:185], v61 offset:26624\n\t"
    "ds_read_b64_tr_b16 v[186:187], v61 offset:33280\n\t"
    "ds_read_b64_tr_b16 v[36:37], v102\n\t"
    "ds_read_b64_tr_b16 v[38:39], v102 offset:2048\n\t"
    "ds_read_b64_tr_b16 v[48:49], v103\n\t"
    "ds_read_b64_tr_b16 v[50:51], v103 offset:2048\n\t"
    "ds_read_b64_tr_b16 v[40:41], v102 offset:4096\n\t"
    "ds_read_b64_tr_b16 v[42:43], v102 offset:6144\n\t"
    "ds_read_b64_tr_b16 v[52:53], v103 offset:4096\n\t"
    "ds_read_b64_tr_b16 v[54:55], v103 offset:6144\n\t"
    "ds_read_b64_tr_b16 v[44:45], v102 offset:8192\n\t"
    "ds_read_b64_tr_b16 v[46:47], v102 offset:10240\n\t"
    "ds_read_b64_tr_b16 v[56:57], v103 offset:8192\n\t"
    "ds_read_b64_tr_b16 v[58:59], v103 offset:10240\n\t"
    "ds_read_b128 v[12:15], v60\n\t"
    "ds_read_b128 v[16:19], v60 offset:13312\n\t"
    "ds_read_b128 v[20:23], v60 offset:26624\n\t"
    "s_waitcnt lgkmcnt(15)\n\t"
    "v_mfma_f32_16x16x32_bf16 v[216:219], v[176:179], v[176:179], 0\n\t"
    "v_mfma_f32_16x16x32_bf16 v[212:215], v[152:155], v[176:179], 0\n\t"
    "v_lshlrev_b32 v188, 16, v176\n\t"
    "v_and_b32 v189, 0xffff0000, v176\n\t"
    "v_lshlrev_b32 v190, 16, v177\n\t"
    "v_and_b32 v191, 0xffff0000, v177\n\t"
    "v_lshlrev_b32 v192, 16, v178\n\t"
    "v_and_b32 v193, 0xffff0000, v178\n\t"
    "v_lshlrev_b32 v194, 16, v179\n\t"
    "v_and_b32 v195, 0xffff0000, v179\n\t"
    "v_mfma_f32_16x16x32_bf16 v[216:219], v[180:183], v[180:183], v[216:219]\n\t"
    "v_mfma_f32_16x16x32_bf16 v[212:215], v[152:155], v[180:183], v[212:215]\n\t"
    "v_lshlrev_b32 v196, 16, v180\n\t"
    "v_and_b32 v197, 0xffff0000, v180\n\t"
    "v_lshlrev_b32 v198, 16, v181\n\t"
    "v_and_b32 v199, 0xffff0000, v181\n\t"
    "v_lshlrev_b32 v200, 16, v182\n\t"
    "v_and_b32 v201, 0xffff0000, v182\n\t"
    "v_lshlrev_b32 v202, 16, v183\n\t"
    "v_and_b32 v203, 0xffff0000, v183\n\t"
    "v_mfma_f32_16x16x32_bf16 v[216:219], v[184:187], v[184:187], v[216:219]\n\t"
    "v_mfma_f32_16x16x32_bf16 v[212:215], v[152:155], v[184:187], v[212:215]\n\t"
    "v_lshlrev_b32 v204, 16, v184\n\t"
    "v_and_b32 v205, 0xffff0000, v184\n\t"
    "v_lshlrev_b32 v206, 16, v185\n\t"
    "v_and_b32 v207, 0xffff0000, v185\n\t"
    "v_lshlrev_b32 v208, 16, v186\n\t"
    "v_and_b32 v209, 0xffff0000, v186\n\t"
    "v_lshlrev_b32 v210, 16, v187\n\t"
    "v_and_b32 v211, 0xffff0000, v187\n\t"
    "v_mul_f32 v216, v216, v168\n\t"
    "v_fmac_f32 v216, v217, v169\n\t"
    "v_fmac_f32 v216, v218, v170\n\t"
    "v_fmac_f32 v216, v219, v171\n\t"
    "v_mul_f32 v212, 0x3c2aaaab, v212\n\t"
    "v_mov_b32 v217, v216\n\t"
    "v_mul_f32 v213, v212, v212\n\t"
    "s_nop 0\n\t"
    "v_permlane16_swap_b32 v216, v217\n\t"
    "v_add_f32 v216, v216, v217\n\t"
    "v_mov_b32 v217, v216\n\t"
    "s_nop 0\n\t"
    "s_nop 0\n\t"
    "v_permlane32_swap_b32 v216, v217\n\t"
    "v_add_f32 v216, v216, v217\n\t"
    "v_fma_f32 v216, v216, s52, -v213\n\t"
    "v_max_f32 v216, 0, v216\n\t"
    "v_fmamk_f32 v216, v216, 0x4091afa4, v116\n\t"
    "v_rsq_f32 v216, v216\n\t"
    "s_nop 0\n\t"
    "v_mul_f32_e64 v212, -v212, v216\n\t"
    "v_pk_fma_f32 v[188:189], v[188:189], v[216:217], v[212:213] op_sel_hi:[1,0,0]\n\t"
    "v_pk_fma_f32 v[190:191], v[190:191], v[216:217], v[212:213] op_sel_hi:[1,0,0]\n\t"
    "v_pk_fma_f32 v[192:193], v[192:193], v[216:217], v[212:213] op_sel_hi:[1,0,0]\n\t"
    "v_pk_fma_f32 v[194:195], v[194:195], v[216:217], v[212:213] op_sel_hi:[1,0,0]\n\t"
    "v_pk_fma_f32 v[196:197], v[196:197], v[216:217], v[212:213] op_sel_hi:[1,0,0]\n\t"
    "v_pk_fma_f32 v[198:199], v[198:199], v[216:217], v[212:213] op_sel_hi:[1,0,0]\n\t"
    "v_pk_fma_f32 v[200:201], v[200:201], v[216:217], v[212:213] op_sel_hi:[1,0,0]\n\t"
    "v_pk_fma_f32 v[202:203], v[202:203], v[216:217], v[212:213] op_sel_hi:[1,0,0]\n\t"
    "v_pk_fma_f32 v[204:205], v[204:205], v[216:217], v[212:213] op_sel_hi:[1,0,0]\n\t"
    "v_pk_fma_f32 v[206:207], v[206:207], v[216:217], v[212:213] op_sel_hi:[1,0,0]\n\t"
    "v_pk_fma_f32 v[208:209], v[208:209], v[216:217], v[212:213] op_sel_hi:[1,0,0]\n\t"
    "v_pk_fma_f32 v[210:211], v[210:211], v[216:217], v[212:213] op_sel_hi:[1,0,0]\n\t"
    "v_cvt_pk_bf16_f32 v24, v188, v189\n\t"
    "v_cvt_pk_bf16_f32 v25, v190, v191\n\t"
    "v_cvt_pk_bf16_f32 v26, v192, v193\n\t"
    "v_cvt_pk_bf16_f32 v27, v194, v195\n\t"
    "v_cvt_pk_bf16_f32 v28, v196, v197\n\t"
    "v_cvt_pk_bf16_f32 v29, v198, v199\n\t"
    "v_cvt_pk_bf16_f32 v30, v200, v201\n\t"
    "v_cvt_pk_bf16_f32 v31, v202, v203\n\t"
    "v_cvt_pk_bf16_f32 v32, v204, v205\n\t"
    "v_cvt_pk_bf16_f32 v33, v206, v207\n\t"
    "v_cvt_pk_bf16_f32 v34, v208, v209\n\t"
    "v_cvt_pk_bf16_f32 v35, v210, v211\n\t"
    "ds_write_b128 v136, v[24:27] offset:256\n\t"
    "ds_write_b128 v136, v[28:31] offset:13568\n\t"
    "ds_write_b128 v136, v[32:35] offset:26880\n\t"
    "s_branch 9f\n\t"
    "8:\n\t"
    "v_mbcnt_lo_u32_b32 v62, -1, 0\n\t"
    "v_mbcnt_hi_u32_b32 v62, -1, v62\n\t"
    "v_and_b32 v63, 3, v62\n\t"
    "v_min_u32 v63, 2, v63\n\t"
    "v_mul_u32_u24 v64, 13312, v63\n\t"
    "v_and_b32 v61, -32, v83\n\t"
    "v_add_u32 v61, v61, v64\n\t"
    "ds_read_b64_tr_b16 v[176:177], v61 offset:384\n\t"
    "ds_read_b64_tr_b16 v[178:179], v61 offset:7040\n\t"
    "ds_read_b64_tr_b16 v[36:37], v102\n\t"
    "ds_read_b64_tr_b16 v[38:39], v102 offset:2048\n\t"
    "ds_read_b64_tr_b16 v[48:49], v103\n\t"
    "ds_read_b64_tr_b16 v[50:51], v103 offset:2048\n\t"
    "ds_read_b64_tr_b16 v[40:41], v102 offset:4096\n\t"
    "ds_read_b64_tr_b16 v[42:43], v102 offset:6144\n\t"
    "ds_read_b64_tr_b16 v[52:53], v103 offset:4096\n\t"
    "ds_read_b64_tr_b16 v[54:55], v103 offset:6144\n\t"
    "ds_read_b64_tr_b16 v[44:45], v102 offset:8192\n\t"
    "ds_read_b64_tr_b16 v[46:47], v102 offset:10240\n\t"
    "ds_read_b64_tr_b16 v[56:57], v103 offset:8192\n\t"
    "ds_read_b64_tr_b16 v[58:59], v103 offset:10240\n\t"
    "ds_read_b128 v[12:15], v60\n\t"
    "ds_read_b128 v[16:19], v60 offset:13312\n\t"
    "ds_read_b128 v[20:23], v60 offset:26624\n\t"
    "v_bfe_u32 v63, v62, 2, 2\n\t"
    "v_cmp_ne_u32 vcc, 3, v63\n\t"
    "v_mul_u32_u24 v63, 272, v63\n\t"
    "v_add_u32 v63, v63, v85\n\t"
    "v_cndmask_b32 v196, 0, 1.0, vcc\n\t"
    "s_waitcnt lgkmcnt(15)\n\t"
    "v_mfma_f32_16x16x32_bf16 v[192:195], v[176:179], v[176:179], 0\n\t"
    "v_mfma_f32_16x16x32_bf16 v[188:191], v[152:155], v[176:179], 0\n\t"
    "v_lshlrev_b32 v180, 16, v176\n\t"
    "v_and_b32 v181, 0xffff0000, v176\n\t"
    "v_lshlrev_b32 v182, 16, v177\n\t"
    "v_and_b32 v183, 0xffff0000, v177\n\t"
    "v_lshlrev_b32 v184, 16, v178\n\t"
    "v_and_b32 v185, 0xffff0000, v178\n\t"
    "v_lshlrev_b32 v186, 16, v179\n\t"
    "v_and_b32 v187, 0xffff0000, v179\n\t"
    "v_mul_f32 v192, v192, v168\n\t"
    "v_fmac_f32 v192, v193, v169\n\t"
    "v_fmac_f32 v192, v194, v170\n\t"
    "v_fmac_f32 v192, v195, v171\n\t"
    "v_mul_f32 v188, v188, v196\n\t"
    "v_mul_f32 v192, v192, v196\n\t"
    "s_nop 0\n\t"
    "v_add_f32_dpp v188, v188, v188 row_ror:4 row_mask:0xf bank_mask:0xf\n\t"
    "v_add_f32_dpp v192, v192, v192 row_ror:4 row_mask:0xf bank_mask:0xf\n\t"
    "s_nop 0\n\t"
    "v_add_f32_dpp v188, v188, v188 row_ror:8 row_mask:0xf bank_mask:0xf\n\t"
    "v_add_f32_dpp v192, v192, v192 row_ror:8 row_mask:0xf bank_mask:0xf\n\t"
    "v_mul_f32 v188, 0x3c2aaaab, v188\n\t"
    "v_mov_b32 v193, v192\n\t"
    "s_nop 1\n\t"
    "v_permlane16_swap_b32 v192, v193\n\t"
    "v_add_f32 v192, v192, v193\n\t"
    "v_mov_b32 v193, v192\n\t"
    "s_nop 0\n\t"
    "s_nop 0\n\t"
    "v_permlane32_swap_b32 v192, v193\n\t"
    "v_add_f32 v192, v192, v193\n\t"
    "v_mul_f32 v189, v188, v188\n\t"
    "v_fma_f32 v192, v192, s52, -v189\n\t"
    "v_max_f32 v192, 0, v192\n\t"
    "v_fmamk_f32 v192, v192, 0x4091afa4, v116\n\t"
    "v_rsq_f32 v192, v192\n\t"
    "s_nop 0\n\t"
    "v_mul_f32_e64 v188, -v188, v192\n\t"
    "v_pk_fma_f32 v[180:181], v[180:181], v[192:193], v[188:189] op_sel_hi:[1,0,0]\n\t"
    "v_pk_fma_f32 v[182:183], v[182:183], v[192:193], v[188:189] op_sel_hi:[1,0,0]\n\t"
    "v_pk_fma_f32 v[184:185], v[184:185], v[192:193], v[188:189] op_sel_hi:[1,0,0]\n\t"
    "v_pk_fma_f32 v[186:187], v[186:187], v[192:193], v[188:189] op_sel_hi:[1,0,0]\n\t"
    "v_cvt_pk_bf16_f32 v176, v180, v181\n\t"
    "v_cvt_pk_bf16_f32 v177, v182, v183\n\t"
    "v_cvt_pk_bf16_f32 v178, v184, v185\n\t"
    "v_cvt_pk_bf16_f32 v179, v186, v187\n\t"
    "s_mov_b32 exec_lo, 0x0fff0fff\n\t"
    "s_mov_b32 exec_hi, 0x0fff0fff\n\t"
    "ds_write_b128 v63, v[176:179]\n\t"
    "s_mov_b64 exec, -1\n\t"
    "s_branch 9f\n\t"
    "7:\n\t"
    "ds_read_b64_tr_b16 v[36:37], v102\n\t"
    "ds_read_b64_tr_b16 v[38:39], v102 offset:2048\n\t"
    "ds_read_b64_tr_b16 v[48:49], v103\n\t"
    "ds_read_b64_tr_b16 v[50:51], v103 offset:2048\n\t"
    "ds_read_b64_tr_b16 v[40:41], v102 offset:4096\n\t"
    "ds_read_b64_tr_b16 v[42:43], v102 offset:6144\n\t"
    "ds_read_b64_tr_b16 v[52:53], v103 offset:4096\n\t"
    "ds_read_b64_tr_b16 v[54:55], v103 offset:6144\n\t"
    "ds_read_b64_tr_b16 v[44:45], v102 offset:8192\n\t"
    "ds_read_b64_tr_b16 v[46:47], v102 offset:10240\n\t"
    "ds_read_b64_tr_b16 v[56:57], v103 offset:8192\n\t"
    "ds_read_b64_tr_b16 v[58:59], v103 offset:10240\n\t"
    "ds_read_b128 v[12:15], v60\n\t"
    "ds_read_b128 v[16:19], v60 offset:13312\n\t"
    "ds_read_b128 v[20:23], v60 offset:26624\n\t"
    "s_cmp_lt_u32 s36, 5\n\t"
    "s_cbranch_scc1 45f\n\t"
    "v_mbcnt_lo_u32_b32 v62, -1, 0\n\t"
    "v_mbcnt_hi_u32_b32 v62, -1, v62\n\t"
    "s_mul_i32 s78, s36, 128\n\t"
    "s_add_u32 s78, s78, -640\n\t"
    "v_add_u32 v63, s78, v62\n\t"
    "v_min_u32 v63, 0x125, v63\n\t"
    "v_mul_u32_u24 v64, 0xc31, v63\n\t"
    "v_lshrrev_b32 v64, 16, v64\n\t"
    "v_mul_u32_u24 v64, 40320, v64\n\t"
    "v_lshl_add_u32 v64, v63, 7, v64\n\t"
    "buffer_load_dword v248, v64, s[24:27], s43 offen\n\t"
    "s_mul_i32 s78, s36, 128\n\t"
    "s_add_u32 s78, s78, -576\n\t"
    "v_add_u32 v63, s78, v62\n\t"
    "v_min_u32 v63, 0x125, v63\n\t"
    "v_mul_u32_u24 v64, 0xc31, v63\n\t"
    "v_lshrrev_b32 v64, 16, v64\n\t"
    "v_mul_u32_u24 v64, 40320, v64\n\t"
    "v_lshl_add_u32 v64, v63, 7, v64\n\t"
    "buffer_load_dword v248, v64, s[24:27], s43 offen\n\t"
    "s_mul_i32 s77, s42, 64\n\t"
    "s_add_u32 s77, s77, 4096\n\t"
    "s_and_b32 s78, s42, 7\n\t"
    "s_mul_i32 s78, s78, 4096\n\t"
    "s_add_u32 s78, s78, 790528\n\t"
    "s_cmp_eq_u32 s36, 5\n\t"
    "s_cselect_b32 s79, 14, 7\n\t"
    "s_cselect_b32 s78, s77, s78\n\t"
    "v_min_u32 v62, 47, v62\n\t"
    "v_lshlrev_b32 v64, s79, v62\n\t"
    "buffer_load_dword v248, v64, s[16:19], s78 offen sc1\n\t"
    "v_min_u32 v62, 11, v62\n\t"
    "v_lshlrev_b32 v64, 6, v62\n\t"
    "global_load_dword v248, v64, s[28:29]\n\t"
    "global_load_dword v248, v64, s[30:31]\n\t"
    "45:\n\t"
    "9:\n\t"
    "s_waitcnt lgkmcnt(0)\n\t"
    "v_mfma_f32_16x16x32_bf16 v[108:111], v[36:39], v[88:91], 0\n\t"
    "v_mfma_f32_16x16x32_bf16 v[112:115], v[48:51], v[88:91], 0\n\t"
    "v_mfma_f32_16x16x32_bf16 v[108:111], v[40:43], v[92:95], v[108:111]\n\t"
    "v_mfma_f32_16x16x32_bf16 v[112:115], v[52:55], v[92:95], v[112:115]\n\t"
    "v_mfma_f32_16x16x32_bf16 v[108:111], v[44:47], v[96:99], v[108:111]\n\t"
    "v_mfma_f32_16x16x32_bf16 v[112:115], v[56:59], v[96:99], v[112:115]\n\t"
    "v_readfirstlane_b32 s41, v250\n\t"
    "v_readfirstlane_b32 s40, v251\n\t"
    "v_add_u32 v0, s35, v84\n\t"
    "v_mov_b32 v8, v87\n\t"
    "v_mov_b32 v9, v87\n\t"
    "v_mov_b32 v10, v87\n\t"
    "v_mov_b32 v11, v87\n\t"
    "v_mov_b32 v244, v252\n\t"
    "v_mov_b32 v245, v253\n\t"
    "v_mov_b32 v246, v254\n\t"
    "s_mul_i32 s40, s40, 0x4c957f2d\n\t"
    "s_mul_i32 s78, s41, 0x5851f42d\n\t"
    "s_mul_hi_u32 s79, s41, 0x4c957f2d\n\t"
    "s_mul_i32 s41, s41, 0x4c957f2d\n\t"
    "s_add_u32 s40, s40, s78\n\t"
    "s_add_u32 s40, s40, s79\n\t"
    "s_add_u32 s41, s41, 0xf767814f\n\t"
    "s_addc_u32 s40, s40, 0x14057b7e\n\t"
    "s_lshr_b32 s39, s41, 23\n\t"
    "s_setprio 3\n\t"
    "v_pk_fma_f32 v[60:61], v[108:109], v[108:109], s[50:51] neg_lo:[0,1,0] neg_hi:[0,1,0]\n\t"
    "v_pk_fma_f32 v[62:63], v[110:111], v[110:111], s[50:51] neg_lo:[0,1,0] neg_hi:[0,1,0]\n\t"
    "v_pk_fma_f32 v[64:65], v[112:113], v[112:113], s[50:51] neg_lo:[0,1,0] neg_hi:[0,1,0]\n\t"
    "v_pk_fma_f32 v[66:67], v[114:115], v[114:115], s[50:51] neg_lo:[0,1,0] neg_hi:[0,1,0]\n\t"
    "v_pk_mul_f32 v[60:61], v[60:61], v[108:109]\n\t"
    "v_pk_mul_f32 v[62:63], v[62:63], v[110:111]\n\t"
    "v_pk_mul_f32 v[64:65], v[64:65], v[112:113]\n\t"
    "v_pk_mul_f32 v[66:67], v[66:67], v[114:115]\n\t"
    "v_mfma_f32_16x16x32_bf16 v[156:159], v[36:39], v[12:15], 0\n\t"
    "v_exp_f32 v60, v60\n\t"
    "v_exp_f32 v61, v61\n\t"
    "v_mfma_f32_16x16x32_bf16 v[160:163], v[48:51], v[12:15], 0\n\t"
    "v_exp_f32 v62, v62\n\t"
    "v_exp_f32 v63, v63\n\t"
    "v_mfma_f32_16x16x32_bf16 v[156:159], v[40:43], v[16:19], v[156:159]\n\t"
    "v_exp_f32 v64, v64\n\t"
    "v_exp_f32 v65, v65\n\t"
    "v_exp_f32 v66, v66\n\t"
    "v_exp_f32 v67, v67\n\t"
    "v_pk_add_f32 v[60:61], v[60:61], 1.0 op_sel_hi:[1,0]\n\t"
    "v_pk_add_f32 v[62:63], v[62:63], 1.0 op_sel_hi:[1,0]\n\t"
    "v_pk_add_f32 v[64:65], v[64:65], 1.0 op_sel_hi:[1,0]\n\t"
    "v_pk_add_f32 v[66:67], v[66:67], 1.0 op_sel_hi:[1,0]\n\t"
    "v_mfma_f32_16x16x32_bf16 v[160:163], v[52:55], v[16:19], v[160:163]\n\t"
    "v_rcp_f32 v60, v60\n\t"
    "v_rcp_f32 v61, v61\n\t"
    "v_mfma_f32_16x16x32_bf16 v[156:159], v[44:47], v[20:23], v[156:159]\n\t"
    "v_rcp_f32 v62, v62\n\t"
    "v_rcp_f32 v63, v63\n\t"
    "v_mfma_f32_16x16x32_bf16 v[160:163], v[56:59], v[20:23], v[160:163]\n\t"
    "v_rcp_f32 v64, v64\n\t"
    "v_rcp_f32 v65, v65\n\t"
    "v_rcp_f32 v66, v66\n\t"
    "v_rcp_f32 v67, v67\n\t"
    "v_pk_mul_f32 v[108:109], v[108:109], v[60:61]\n\t"
    "v_pk_mul_f32 v[110:111], v[110:111], v[62:63]\n\t"
    "v_pk_mul_f32 v[112:113], v[112:113], v[64:65]\n\t"
    "v_pk_mul_f32 v[114:115], v[114:115], v[66:67]\n\t"
    "v_pk_mul_f32 v[76:77], v[108:109], v[108:109]\n\t"
    "v_pk_mul_f32 v[78:79], v[110:111], v[110:111]\n\t"
    "v_pk_mul_f32 v[80:81], v[112:113], v[112:113]\n\t"
    "v_pk_mul_f32 v[82:83], v[114:115], v[114:115]\n\t"
    "s_waitcnt lgkmcnt(0)\n\t"
    "s_barrier\n\t"
    "ds_read_b128 v[24:27], v0 offset:256\n\t"
    "ds_read_b128 v[28:31], v0 offset:13568\n\t"
    "ds_read_b128 v[32:35], v0 offset:26880\n\t"
    "v_mov_b32 v84, 88320\n\t"
    "s_mov_b32 exec_lo, 0x000f000f\n\t"
    "s_mov_b32 exec_hi, 0x000f000f\n\t"
    "v_mov_b32 v84, v106\n\t"
    "s_mov_b64 exec, -1\n\t"
    "ds_read_b128 v[148:151], v84\n\t"
    "ds_read_b128 v[152:155], v84 offset:272\n\t"
    "ds_read_b128 v[196:199], v84 offset:544\n\t"
    "v_mov_b32 v85, 88320\n\t"
    "s_mov_b32 exec_lo, 0x00f000f0\n\t"
    "s_mov_b32 exec_hi, 0x00f000f0\n\t"
    "v_mov_b32 v85, v106\n\t"
    "s_mov_b64 exec, -1\n\t"
    "ds_read_b128 v[200:203], v85\n\t"
    "ds_read_b128 v[140:143], v85 offset:272\n\t"
    "ds_read_b128 v[144:147], v85 offset:544\n\t"
    "v_xor_b32 v102, s61, v100\n\t"
    "v_xor_b32 v103, s61, v101\n\t"
    "v_pk_fma_f32 v[60:61], v[156:157], v[156:157], s[50:51] neg_lo:[0,1,0] neg_hi:[0,1,0]\n\t"
    "v_pk_fma_f32 v[62:63], v[158:159], v[158:159], s[50:51] neg_lo:[0,1,0] neg_hi:[0,1,0]\n\t"
    "v_pk_fma_f32 v[64:65], v[160:161], v[160:161], s[50:51] neg_lo:[0,1,0] neg_hi:[0,1,0]\n\t"
    "v_pk_fma_f32 v[66:67], v[162:163], v[162:163], s[50:51] neg_lo:[0,1,0] neg_hi:[0,1,0]\n\t"
    "v_pk_mul_f32 v[60:61], v[60:61], v[156:157]\n\t"
    "v_pk_mul_f32 v[62:63], v[62:63], v[158:159]\n\t"
    "v_pk_mul_f32 v[64:65], v[64:65], v[160:161]\n\t"
    "v_pk_mul_f32 v[66:67], v[66:67], v[162:163]\n\t"
    "s_waitcnt lgkmcnt(8)\n\t"
    "v_mfma_f32_16x16x32_bf16 v[204:207], v[36:39], v[24:27], 0\n\t"
    "v_exp_f32 v60, v60\n\t"
    "v_exp_f32 v61, v61\n\t"
    "v_mfma_f32_16x16x32_bf16 v[208:211], v[48:51], v[24:27], 0\n\t"
    "v_exp_f32 v62, v62\n\t"
    "v_exp_f32 v63, v63\n\t"
    "s_waitcnt lgkmcnt(7)\n\t"
    "v_mfma_f32_16x16x32_bf16 v[204:207], v[40:43], v[28:31], v[204:207]\n\t"
    "v_exp_f32 v64, v64\n\t"
    "v_exp_f32 v65, v65\n\t"
    "v_exp_f32 v66, v66\n\t"
    "v_exp_f32 v67, v67\n\t"
    "v_pk_add_f32 v[60:61], v[60:61], 1.0 op_sel_hi:[1,0]\n\t"
    "v_pk_add_f32 v[62:63], v[62:63], 1.0 op_sel_hi:[1,0]\n\t"
    "v_pk_add_f32 v[64:65], v[64:65], 1.0 op_sel_hi:[1,0]\n\t"
    "v_pk_add_f32 v[66:67], v[66:67], 1.0 op_sel_hi:[1,0]\n\t"
    "v_mfma_f32_16x16x32_bf16 v[208:211], v[52:55], v[28:31], v[208:211]\n\t"
    "v_rcp_f32 v60, v60\n\t"
    "v_rcp_f32 v61, v61\n\t"
    "s_waitcnt lgkmcnt(6)\n\t"
    "v_mfma_f32_16x16x32_bf16 v[204:207], v[44:47], v[32:35], v[204:207]\n\t"
    "v_rcp_f32 v62, v62\n\t"
    "v_rcp_f32 v63, v63\n\t"
    "v_mfma_f32_16x16x32_bf16 v[208:211], v[56:59], v[32:35], v[208:211]\n\t"
    "v_rcp_f32 v64, v64\n\t"
    "v_rcp_f32 v65, v65\n\t"
    "v_rcp_f32 v66, v66\n\t"
    "v_rcp_f32 v67, v67\n\t"
    "v_pk_mul_f32 v[156:157], v[156:157], v[60:61]\n\t"
    "v_pk_mul_f32 v[158:159], v[158:159], v[62:63]\n\t"
    "v_pk_mul_f32 v[160:161], v[160:161], v[64:65]\n\t"
    "v_pk_mul_f32 v[162:163], v[162:163], v[66:67]\n\t"
    "v_pk_fma_f32 v[76:77], v[156:157], v[156:157], v[76:77]\n\t"
    "v_pk_fma_f32 v[78:79], v[158:159], v[158:159], v[78:79]\n\t"
    "v_pk_fma_f32 v[80:81], v[160:161], v[160:161], v[80:81]\n\t"
    "v_pk_fma_f32 v[82:83], v[162:163], v[162:163], v[82:83]\n\t"
    "s_waitcnt lgkmcnt(5)\n\t"
    "v_mfma_f32_16x16x32_bf16 v[252:255], v[36:39], v[148:151], 0\n\t"
    "ds_read_b64_tr_b16 v[36:37], v102\n\t"
    "ds_read_b64_tr_b16 v[38:39], v102 offset:2048\n\t"
    "s_waitcnt lgkmcnt(4)\n\t"
    "v_mfma_f32_16x16x32_bf16 v[252:255], v[48:51], v[200:203], v[252:255]\n\t"
    "ds_read_b64_tr_b16 v[48:49], v103\n\t"
    "ds_read_b64_tr_b16 v[50:51], v103 offset:2048\n\t"
    "v_mfma_f32_16x16x32_bf16 v[252:255], v[40:43], v[152:155], v[252:255]\n\t"
    "ds_read_b64_tr_b16 v[40:41], v102 offset:4096\n\t"
    "ds_read_b64_tr_b16 v[42:43], v102 offset:6144\n\t"
    "s_waitcnt lgkmcnt(7)\n\t"
    "v_mfma_f32_16x16x32_bf16 v[252:255], v[52:55], v[140:143], v[252:255]\n\t"
    "ds_read_b64_tr_b16 v[52:53], v103 offset:4096\n\t"
    "ds_read_b64_tr_b16 v[54:55], v103 offset:6144\n\t"
    "v_mfma_f32_16x16x32_bf16 v[252:255], v[44:47], v[196:199], v[252:255]\n\t"
    "ds_read_b64_tr_b16 v[44:45], v102 offset:8192\n\t"
    "ds_read_b64_tr_b16 v[46:47], v102 offset:10240\n\t"
    "s_waitcnt lgkmcnt(10)\n\t"
    "v_mfma_f32_16x16x32_bf16 v[252:255], v[56:59], v[144:147], v[252:255]\n\t"
    "ds_read_b64_tr_b16 v[56:57], v103 offset:8192\n\t"
    "ds_read_b64_tr_b16 v[58:59], v103 offset:10240\n\t"
    "s_barrier\n\t"
    "v_pk_fma_f32 v[60:61], v[204:205], v[204:205], s[50:51] neg_lo:[0,1,0] neg_hi:[0,1,0]\n\t"
    "v_pk_fma_f32 v[62:63], v[206:207], v[206:207], s[50:51] neg_lo:[0,1,0] neg_hi:[0,1,0]\n\t"
    "v_pk_fma_f32 v[64:65], v[208:209], v[208:209], s[50:51] neg_lo:[0,1,0] neg_hi:[0,1,0]\n\t"
    "v_pk_fma_f32 v[66:67], v[210:211], v[210:211], s[50:51] neg_lo:[0,1,0] neg_hi:[0,1,0]\n\t"
    "v_pk_mul_f32 v[60:61], v[60:61], v[204:205]\n\t"
    "v_pk_mul_f32 v[62:63], v[62:63], v[206:207]\n\t"
    "v_pk_mul_f32 v[64:65], v[64:65], v[208:209]\n\t"
    "v_pk_mul_f32 v[66:67], v[66:67], v[210:211]\n\t"
    "s_waitcnt lgkmcnt(10)\n\t"
    "v_mfma_f32_16x16x32_bf16 v[116:119], v[36:39], v[88:91], 0\n\t"
    "v_exp_f32 v60, v60\n\t"
    "v_exp_f32 v61, v61\n\t"
    "s_waitcnt lgkmcnt(8)\n\t"
    "v_mfma_f32_16x16x32_bf16 v[120:123], v[48:51], v[88:91], 0\n\t"
    "v_exp_f32 v62, v62\n\t"
    "v_exp_f32 v63, v63\n\t"
    "s_waitcnt lgkmcnt(6)\n\t"
    "v_mfma_f32_16x16x32_bf16 v[116:119], v[40:43], v[92:95], v[116:119]\n\t"
    "v_exp_f32 v64, v64\n\t"
    "v_exp_f32 v65, v65\n\t"
    "v_exp_f32 v66, v66\n\t"
    "v_exp_f32 v67, v67\n\t"
    "v_pk_add_f32 v[60:61], v[60:61], 1.0 op_sel_hi:[1,0]\n\t"
    "v_pk_add_f32 v[62:63], v[62:63], 1.0 op_sel_hi:[1,0]\n\t"
    "v_pk_add_f32 v[64:65], v[64:65], 1.0 op_sel_hi:[1,0]\n\t"
    "v_pk_add_f32 v[66:67], v[66:67], 1.0 op_sel_hi:[1,0]\n\t"
    "s_waitcnt lgkmcnt(4)\n\t"
    "v_mfma_f32_16x16x32_bf16 v[120:123], v[52:55], v[92:95], v[120:123]\n\t"
    "v_rcp_f32 v60, v60\n\t"
    "v_rcp_f32 v61, v61\n\t"
    "s_waitcnt lgkmcnt(2)\n\t"
    "v_mfma_f32_16x16x32_bf16 v[116:119], v[44:47], v[96:99], v[116:119]\n\t"
    "v_rcp_f32 v62, v62\n\t"
    "v_rcp_f32 v63, v63\n\t"
    "s_waitcnt lgkmcnt(0)\n\t"
    "v_mfma_f32_16x16x32_bf16 v[120:123], v[56:59], v[96:99], v[120:123]\n\t"
    "v_rcp_f32 v64, v64\n\t"
    "v_rcp_f32 v65, v65\n\t"
    "s_add_u32 m0, s53, 0\n\t"
    "s_add_u32 s78, s53, 0\n\t"
    "buffer_load_dwordx4 v244, s[12:15], s78 offen lds\n\t"
    "v_rcp_f32 v66, v66\n\t"
    "v_rcp_f32 v67, v67\n\t"
    "v_pk_mul_f32 v[204:205], v[204:205], v[60:61]\n\t"
    "v_pk_mul_f32 v[206:207], v[206:207], v[62:63]\n\t"
    "v_pk_mul_f32 v[208:209], v[208:209], v[64:65]\n\t"
    "v_pk_mul_f32 v[210:211], v[210:211], v[66:67]\n\t"
    "v_pk_fma_f32 v[76:77], v[204:205], v[204:205], v[76:77]\n\t"
    "v_pk_fma_f32 v[78:79], v[206:207], v[206:207], v[78:79]\n\t"
    "v_pk_fma_f32 v[80:81], v[208:209], v[208:209], v[80:81]\n\t"
    "v_pk_fma_f32 v[82:83], v[210:211], v[210:211], v[82:83]\n\t"
    "v_pk_fma_f32 v[60:61], v[116:117], v[116:117], s[50:51] neg_lo:[0,1,0] neg_hi:[0,1,0]\n\t"
    "v_pk_fma_f32 v[62:63], v[118:119], v[118:119], s[50:51] neg_lo:[0,1,0] neg_hi:[0,1,0]\n\t"
    "v_pk_fma_f32 v[64:65], v[120:121], v[120:121], s[50:51] neg_lo:[0,1,0] neg_hi:[0,1,0]\n\t"
    "v_pk_fma_f32 v[66:67], v[122:123], v[122:123], s[50:51] neg_lo:[0,1,0] neg_hi:[0,1,0]\n\t"
    "v_pk_mul_f32 v[60:61], v[60:61], v[116:117]\n\t"
    "v_pk_mul_f32 v[62:63], v[62:63], v[118:119]\n\t"
    "v_pk_mul_f32 v[64:65], v[64:65], v[120:121]\n\t"
    "v_pk_mul_f32 v[66:67], v[66:67], v[122:123]\n\t"
    "v_mfma_f32_16x16x32_bf16 v[164:167], v[36:39], v[12:15], 0\n\t"
    "v_exp_f32 v60, v60\n\t"
    "v_exp_f32 v61, v61\n\t"
    "v_mfma_f32_16x16x32_bf16 v[168:171], v[48:51], v[12:15], 0\n\t"
    "v_exp_f32 v62, v62\n\t"
    "v_exp_f32 v63, v63\n\t"
    "v_mfma_f32_16x16x32_bf16 v[164:167], v[40:43], v[16:19], v[164:167]\n\t"
    "v_exp_f32 v64, v64\n\t"
    "v_exp_f32 v65, v65\n\t"
    "v_exp_f32 v66, v66\n\t"
    "v_exp_f32 v67, v67\n\t"
    "v_pk_add_f32 v[60:61], v[60:61], 1.0 op_sel_hi:[1,0]\n\t"
    "v_pk_add_f32 v[62:63], v[62:63], 1.0 op_sel_hi:[1,0]\n\t"
    "v_pk_add_f32 v[64:65], v[64:65], 1.0 op_sel_hi:[1,0]\n\t"
    "v_pk_add_f32 v[66:67], v[66:67], 1.0 op_sel_hi:[1,0]\n\t"
    "v_mfma_f32_16x16x32_bf16 v[168:171], v[52:55], v[16:19], v[168:171]\n\t"
    "v_rcp_f32 v60, v60\n\t"
    "v_rcp_f32 v61, v61\n\t"
    "v_mfma_f32_16x16x32_bf16 v[164:167], v[44:47], v[20:23], v[164:167]\n\t"
    "v_rcp_f32 v62, v62\n\t"
    "v_rcp_f32 v63, v63\n\t"
    "v_mfma_f32_16x16x32_bf16 v[168:171], v[56:59], v[20:23], v[168:171]\n\t"
    "v_rcp_f32 v64, v64\n\t"
    "v_rcp_f32 v65, v65\n\t"
    "s_add_u32 m0, s53, 1024\n\t"
    "s_add_u32 s78, s53, 0\n\t"
    "buffer_load_dwordx4 v245, s[12:15], s78 offen lds\n\t"
    "v_rcp_f32 v66, v66\n\t"
    "v_rcp_f32 v67, v67\n\t"
    "v_mov_b32 v6, 88320\n\t"
    "s_mov_b32 exec_lo, 0x0f000f00\n\t"
    "s_mov_b32 exec_hi, 0x0f000f00\n\t"
    "v_mov_b32 v6, v106\n\t"
    "s_mov_b64 exec, -1\n\t"
    "ds_read_b128 v[188:191], v6\n\t"
    "ds_read_b128 v[192:195], v6 offset:272\n\t"
    "ds_read_b128 v[236:239], v6 offset:544\n\t"
    "v_mov_b32 v7, 88320\n\t"
    "s_mov_b32 exec_lo, 0xf000f000\n\t"
    "s_mov_b32 exec_hi, 0xf000f000\n\t"
    "v_mov_b32 v7, v106\n\t"
    "s_mov_b64 exec, -1\n\t"
    "ds_read_b128 v[240:243], v7\n\t"
    "ds_read_b128 v[132:135], v7 offset:272\n\t"
    "ds_read_b128 v[136:139], v7 offset:544\n\t"
    "v_pk_mul_f32 v[116:117], v[116:117], v[60:61]\n\t"
    "v_pk_mul_f32 v[118:119], v[118:119], v[62:63]\n\t"
    "v_pk_mul_f32 v[120:121], v[120:121], v[64:65]\n\t"
    "v_pk_mul_f32 v[122:123], v[122:123], v[66:67]\n\t"
    "v_pk_mul_f32 v[68:69], v[116:117], v[116:117]\n\t"
    "v_pk_mul_f32 v[70:71], v[118:119], v[118:119]\n\t"
    "v_pk_mul_f32 v[72:73], v[120:121], v[120:121]\n\t"
    "v_pk_mul_f32 v[74:75], v[122:123], v[122:123]\n\t"
    "s_cmp_eq_u32 s37, 0\n\t"
    "s_cbranch_scc1 1f\n\t"
    "s_waitcnt lgkmcnt(5)\n\t"
    "v_mfma_f32_16x16x32_bf16 v[252:255], v[36:39], v[188:191], v[252:255]\n\t"
    "s_waitcnt lgkmcnt(2)\n\t"
    "v_mfma_f32_16x16x32_bf16 v[252:255], v[48:51], v[240:243], v[252:255]\n\t"
    "v_mfma_f32_16x16x32_bf16 v[252:255], v[40:43], v[192:195], v[252:255]\n\t"
    "s_waitcnt lgkmcnt(1)\n\t"
    "v_mfma_f32_16x16x32_bf16 v[252:255], v[52:55], v[132:135], v[252:255]\n\t"
    "v_mfma_f32_16x16x32_bf16 v[252:255], v[44:47], v[236:239], v[252:255]\n\t"
    "s_waitcnt lgkmcnt(0)\n\t"
    "v_mfma_f32_16x16x32_bf16 v[252:255], v[56:59], v[136:139], v[252:255]\n\t"
    "1:\n\t"
    "v_xor_b32 v102, s62, v100\n\t"
    "v_xor_b32 v103, s62, v101\n\t"
    "v_pk_fma_f32 v[60:61], v[164:165], v[164:165], s[50:51] neg_lo:[0,1,0] neg_hi:[0,1,0]\n\t"
    "v_pk_fma_f32 v[62:63], v[166:167], v[166:167], s[50:51] neg_lo:[0,1,0] neg_hi:[0,1,0]\n\t"
    "v_pk_fma_f32 v[64:65], v[168:169], v[168:169], s[50:51] neg_lo:[0,1,0] neg_hi:[0,1,0]\n\t"
    "v_pk_fma_f32 v[66:67], v[170:171], v[170:171], s[50:51] neg_lo:[0,1,0] neg_hi:[0,1,0]\n\t"
    "v_pk_mul_f32 v[60:61], v[60:61], v[164:165]\n\t"
    "v_pk_mul_f32 v[62:63], v[62:63], v[166:167]\n\t"
    "v_pk_mul_f32 v[64:65], v[64:65], v[168:169]\n\t"
    "v_pk_mul_f32 v[66:67], v[66:67], v[170:171]\n\t"
    "v_mfma_f32_16x16x32_bf16 v[212:215], v[36:39], v[24:27], 0\n\t"
    "ds_read_b64_tr_b16 v[36:37], v102\n\t"
    "ds_read_b64_tr_b16 v[38:39], v102 offset:2048\n\t"
    "v_exp_f32 v60, v60\n\t"
    "v_exp_f32 v61, v61\n\t"
    "v_mfma_f32_16x16x32_bf16 v[216:219], v[48:51], v[24:27], 0\n\t"
    "ds_read_b64_tr_b16 v[48:49], v103\n\t"
    "ds_read_b64_tr_b16 v[50:51], v103 offset:2048\n\t"
    "v_exp_f32 v62, v62\n\t"
    "v_exp_f32 v63, v63\n\t"
    "v_mfma_f32_16x16x32_bf16 v[212:215], v[40:43], v[28:31], v[212:215]\n\t"
    "ds_read_b64_tr_b16 v[40:41], v102 offset:4096\n\t"
    "ds_read_b64_tr_b16 v[42:43], v102 offset:6144\n\t"
    "v_exp_f32 v64, v64\n\t"
    "v_exp_f32 v65, v65\n\t"
    "v_exp_f32 v66, v66\n\t"
    "v_exp_f32 v67, v67\n\t"
    "v_pk_add_f32 v[60:61], v[60:61], 1.0 op_sel_hi:[1,0]\n\t"
    "v_pk_add_f32 v[62:63], v[62:63], 1.0 op_sel_hi:[1,0]\n\t"
    "v_pk_add_f32 v[64:65], v[64:65], 1.0 op_sel_hi:[1,0]\n\t"
    "v_pk_add_f32 v[66:67], v[66:67], 1.0 op_sel_hi:[1,0]\n\t"
    "v_mfma_f32_16x16x32_bf16 v[216:219], v[52:55], v[28:31], v[216:219]\n\t"
    "ds_read_b64_tr_b16 v[52:53], v103 offset:4096\n\t"
    "ds_read_b64_tr_b16 v[54:55], v103 offset:6144\n\t"
    "v_rcp_f32 v60, v60\n\t"
    "v_rcp_f32 v61, v61\n\t"
    "v_mfma_f32_16x16x32_bf16 v[212:215], v[44:47], v[32:35], v[212:215]\n\t"
    "ds_read_b64_tr_b16 v[44:45], v102 offset:8192\n\t"
    "ds_read_b64_tr_b16 v[46:47], v102 offset:10240\n\t"
    "v_rcp_f32 v62, v62\n\t"
    "v_rcp_f32 v63, v63\n\t"
    "v_mfma_f32_16x16x32_bf16 v[216:219], v[56:59], v[32:35], v[216:219]\n\t"
    "ds_read_b64_tr_b16 v[56:57], v103 offset:8192\n\t"
    "ds_read_b64_tr_b16 v[58:59], v103 offset:10240\n\t"
    "v_rcp_f32 v64, v64\n\t"
    "v_rcp_f32 v65, v65\n\t"
    "s_add_u32 m0, s53, 2048\n\t"
    "s_add_u32 s78, s53, 0\n\t"
    "buffer_load_dwordx4 v246, s[12:15], s78 offen lds\n\t"
    "v_rcp_f32 v66, v66\n\t"
    "v_rcp_f32 v67, v67\n\t"
    "v_pk_mul_f32 v[164:165], v[164:165], v[60:61]\n\t"
    "v_pk_mul_f32 v[166:167], v[166:167], v[62:63]\n\t"
    "v_pk_mul_f32 v[168:169], v[168:169], v[64:65]\n\t"
    "v_pk_mul_f32 v[170:171], v[170:171], v[66:67]\n\t"
    "v_pk_fma_f32 v[68:69], v[164:165], v[164:165], v[68:69]\n\t"
    "v_pk_fma_f32 v[70:71], v[166:167], v[166:167], v[70:71]\n\t"
    "v_pk_fma_f32 v[72:73], v[168:169], v[168:169], v[72:73]\n\t"
    "v_pk_fma_f32 v[74:75], v[170:171], v[170:171], v[74:75]\n\t"
    "v_pk_fma_f32 v[60:61], v[252:253], v[252:253], s[50:51] neg_lo:[0,1,0] neg_hi:[0,1,0]\n\t"
    "v_pk_fma_f32 v[62:63], v[254:255], v[254:255], s[50:51] neg_lo:[0,1,0] neg_hi:[0,1,0]\n\t"
    "v_pk_mul_f32 v[60:61], v[60:61], v[252:253]\n\t"
    "v_pk_mul_f32 v[62:63], v[62:63], v[254:255]\n\t"
    "v_exp_f32 v60, v60\n\t"
    "v_exp_f32 v61, v61\n\t"
    "v_exp_f32 v62, v62\n\t"
    "v_exp_f32 v63, v63\n\t"
    "v_pk_add_f32 v[60:61], v[60:61], 1.0 op_sel_hi:[1,0]\n\t"
    "v_pk_add_f32 v[62:63], v[62:63], 1.0 op_sel_hi:[1,0]\n\t"
    "v_rcp_f32 v60, v60\n\t"
    "v_rcp_f32 v61, v61\n\t"
    "v_rcp_f32 v62, v62\n\t"
    "v_rcp_f32 v63, v63\n\t"
    "v_pk_mul_f32 v[252:253], v[252:253], v[60:61]\n\t"
    "v_pk_mul_f32 v[254:255], v[254:255], v[62:63]\n\t"
    "s_mov_b32 exec_lo, 0x000f000f\n\t"
    "s_mov_b32 exec_hi, 0x000f000f\n\t"
    "v_pk_fma_f32 v[76:77], v[252:253], v[252:253], v[76:77]\n\t"
    "v_pk_fma_f32 v[78:79], v[254:255], v[254:255], v[78:79]\n\t"
    "s_mov_b32 exec_lo, 0x00f000f0\n\t"
    "s_mov_b32 exec_hi, 0x00f000f0\n\t"
    "v_pk_fma_f32 v[80:81], v[252:253], v[252:253], v[80:81]\n\t"
    "v_pk_fma_f32 v[82:83], v[254:255], v[254:255], v[82:83]\n\t"
    "s_mov_b32 exec_lo, 0x0f000f00\n\t"
    "s_mov_b32 exec_hi, 0x0f000f00\n\t"
    "v_pk_fma_f32 v[68:69], v[252:253], v[252:253], v[68:69]\n\t"
    "v_pk_fma_f32 v[70:71], v[254:255], v[254:255], v[70:71]\n\t"
    "s_mov_b32 exec_lo, 0xf000f000\n\t"
    "s_mov_b32 exec_hi, 0xf000f000\n\t"
    "v_pk_fma_f32 v[72:73], v[252:253], v[252:253], v[72:73]\n\t"
    "v_pk_fma_f32 v[74:75], v[254:255], v[254:255], v[74:75]\n\t"
    "s_mov_b64 exec, -1\n\t"
    "v_cvt_pk_bf16_f32 v84, v76, v77\n\t"
    "v_cvt_pk_bf16_f32 v85, v78, v79\n\t"
    "v_cvt_pk_bf16_f32 v86, v80, v81\n\t"
    "v_cvt_pk_bf16_f32 v87, v82, v83\n\t"
    "ds_write_b128 v104, v[84:87]\n\t"
    "v_xor_b32 v4, 128, v105\n\t"
    "ds_read_b64_tr_b16 v[0:1], v105\n\t"
    "ds_read_b64_tr_b16 v[2:3], v4\n\t"
    "v_pk_fma_f32 v[60:61], v[212:213], v[212:213], s[50:51] neg_lo:[0,1,0] neg_hi:[0,1,0]\n\t"
    "v_pk_fma_f32 v[62:63], v[214:215], v[214:215], s[50:51] neg_lo:[0,1,0] neg_hi:[0,1,0]\n\t"
    "v_pk_fma_f32 v[64:65], v[216:217], v[216:217], s[50:51] neg_lo:[0,1,0] neg_hi:[0,1,0]\n\t"
    "v_pk_fma_f32 v[66:67], v[218:219], v[218:219], s[50:51] neg_lo:[0,1,0] neg_hi:[0,1,0]\n\t"
    "v_pk_mul_f32 v[60:61], v[60:61], v[212:213]\n\t"
    "v_pk_mul_f32 v[62:63], v[62:63], v[214:215]\n\t"
    "v_pk_mul_f32 v[64:65], v[64:65], v[216:217]\n\t"
    "v_pk_mul_f32 v[66:67], v[66:67], v[218:219]\n\t"
    "s_waitcnt lgkmcnt(13)\n\t"
    "v_mfma_f32_16x16x32_bf16 v[124:127], v[36:39], v[88:91], 0\n\t"
    "v_exp_f32 v60, v60\n\t"
    "v_exp_f32 v61, v61\n\t"
    "s_waitcnt lgkmcnt(11)\n\t"
    "v_mfma_f32_16x16x32_bf16 v[128:131], v[48:51], v[88:91], 0\n\t"
    "v_exp_f32 v62, v62\n\t"
    "v_exp_f32 v63, v63\n\t"
    "s_waitcnt lgkmcnt(9)\n\t"
    "v_mfma_f32_16x16x32_bf16 v[124:127], v[40:43], v[92:95], v[124:127]\n\t"
    "v_exp_f32 v64, v64\n\t"
    "v_exp_f32 v65, v65\n\t"
    "s_waitcnt lgkmcnt(0)\n\t"
    "v_mfma_f32_16x16x32_bf16 v[4:7], v[8:11], v[0:3], 0\n\t"
    "v_exp_f32 v66, v66\n\t"
    "v_exp_f32 v67, v67\n\t"
    "v_pk_add_f32 v[60:61], v[60:61], 1.0 op_sel_hi:[1,0]\n\t"
    "v_pk_add_f32 v[62:63], v[62:63], 1.0 op_sel_hi:[1,0]\n\t"
    "v_pk_add_f32 v[64:65], v[64:65], 1.0 op_sel_hi:[1,0]\n\t"
    "v_pk_add_f32 v[66:67], v[66:67], 1.0 op_sel_hi:[1,0]\n\t"
    "v_mfma_f32_16x16x32_bf16 v[128:131], v[52:55], v[92:95], v[128:131]\n\t"
    "v_rcp_f32 v60, v60\n\t"
    "v_rcp_f32 v61, v61\n\t"
    "v_mfma_f32_16x16x32_bf16 v[124:127], v[44:47], v[96:99], v[124:127]\n\t"
    "v_rcp_f32 v62, v62\n\t"
    "v_rcp_f32 v63, v63\n\t"
    "v_mfma_f32_16x16x32_bf16 v[128:131], v[56:59], v[96:99], v[128:131]\n\t"
    "v_rcp_f32 v64, v64\n\t"
    "v_rcp_f32 v65, v65\n\t"
    "s_add_u32 m0, s53, 3072\n\t"
    "s_add_u32 s78, s53, 3072\n\t"
    "buffer_load_dwordx4 v244, s[12:15], s78 offen lds\n\t"
    "v_rcp_f32 v66, v66\n\t"
    "v_rcp_f32 v67, v67\n\t"
    "v_cvt_pk_bf16_f32 v4, v4, v4\n\t"
    "v_lshl_add_u32 v5, s66, 8, v107\n\t"
    "ds_write_b16 v5, v4\n\t"
    "v_pk_mul_f32 v[212:213], v[212:213], v[60:61]\n\t"
    "v_pk_mul_f32 v[214:215], v[214:215], v[62:63]\n\t"
    "v_pk_mul_f32 v[216:217], v[216:217], v[64:65]\n\t"
    "v_pk_mul_f32 v[218:219], v[218:219], v[66:67]\n\t"
    "v_pk_fma_f32 v[68:69], v[212:213], v[212:213], v[68:69]\n\t"
    "v_pk_fma_f32 v[70:71], v[214:215], v[214:215], v[70:71]\n\t"
    "v_pk_fma_f32 v[72:73], v[216:217], v[216:217], v[72:73]\n\t"
    "v_pk_fma_f32 v[74:75], v[218:219], v[218:219], v[74:75]\n\t"
    "v_cvt_pk_bf16_f32 v84, v68, v69\n\t"
    "v_cvt_pk_bf16_f32 v85, v70, v71\n\t"
    "v_cvt_pk_bf16_f32 v86, v72, v73\n\t"
    "v_cvt_pk_bf16_f32 v87, v74, v75\n\t"
    "ds_write_b128 v104, v[84:87]\n\t"
    "v_xor_b32 v4, 128, v105\n\t"
    "ds_read_b64_tr_b16 v[0:1], v105\n\t"
    "ds_read_b64_tr_b16 v[2:3], v4\n\t"
    "v_pk_fma_f32 v[60:61], v[124:125], v[124:125], s[50:51] neg_lo:[0,1,0] neg_hi:[0,1,0]\n\t"
    "v_pk_fma_f32 v[62:63], v[126:127], v[126:127], s[50:51] neg_lo:[0,1,0] neg_hi:[0,1,0]\n\t"
    "v_pk_fma_f32 v[64:65], v[128:129], v[128:129], s[50:51] neg_lo:[0,1,0] neg_hi:[0,1,0]\n\t"
    "v_pk_fma_f32 v[66:67], v[130:131], v[130:131], s[50:51] neg_lo:[0,1,0] neg_hi:[0,1,0]\n\t"
    "v_pk_mul_f32 v[60:61], v[60:61], v[124:125]\n\t"
    "v_pk_mul_f32 v[62:63], v[62:63], v[126:127]\n\t"
    "v_pk_mul_f32 v[64:65], v[64:65], v[128:129]\n\t"
    "v_pk_mul_f32 v[66:67], v[66:67], v[130:131]\n\t"
    "v_mfma_f32_16x16x32_bf16 v[172:175], v[36:39], v[12:15], 0\n\t"
    "v_exp_f32 v60, v60\n\t"
    "v_exp_f32 v61, v61\n\t"
    "v_mfma_f32_16x16x32_bf16 v[176:179], v[48:51], v[12:15], 0\n\t"
    "v_exp_f32 v62, v62\n\t"
    "v_exp_f32 v63, v63\n\t"
    "v_mfma_f32_16x16x32_bf16 v[172:175], v[40:43], v[16:19], v[172:175]\n\t"
    "v_exp_f32 v64, v64\n\t"
    "v_exp_f32 v65, v65\n\t"
    "s_waitcnt lgkmcnt(0)\n\t"
    "v_mfma_f32_16x16x32_bf16 v[4:7], v[8:11], v[0:3], 0\n\t"
    "v_exp_f32 v66, v66\n\t"
    "v_exp_f32 v67, v67\n\t"
    "v_pk_add_f32 v[60:61], v[60:61], 1.0 op_sel_hi:[1,0]\n\t"
    "v_pk_add_f32 v[62:63], v[62:63], 1.0 op_sel_hi:[1,0]\n\t"
    "v_pk_add_f32 v[64:65], v[64:65], 1.0 op_sel_hi:[1,0]\n\t"
    "v_pk_add_f32 v[66:67], v[66:67], 1.0 op_sel_hi:[1,0]\n\t"
    "v_mfma_f32_16x16x32_bf16 v[176:179], v[52:55], v[16:19], v[176:179]\n\t"
    "v_rcp_f32 v60, v60\n\t"
    "v_rcp_f32 v61, v61\n\t"
    "v_mfma_f32_16x16x32_bf16 v[172:175], v[44:47], v[20:23], v[172:175]\n\t"
    "v_rcp_f32 v62, v62\n\t"
    "v_rcp_f32 v63, v63\n\t"
    "v_mfma_f32_16x16x32_bf16 v[176:179], v[56:59], v[20:23], v[176:179]\n\t"
    "v_rcp_f32 v64, v64\n\t"
    "v_rcp_f32 v65, v65\n\t"
    "s_add_u32 m0, s53, 4096\n\t"
    "s_add_u32 s78, s53, 3072\n\t"
    "buffer_load_dwordx4 v245, s[12:15], s78 offen lds\n\t"
    "v_rcp_f32 v66, v66\n\t"
    "v_rcp_f32 v67, v67\n\t"
    "v_cvt_pk_bf16_f32 v4, v4, v4\n\t"
    "v_lshl_add_u32 v5, s67, 8, v107\n\t"
    "ds_write_b16 v5, v4\n\t"
    "v_pk_mul_f32 v[124:125], v[124:125], v[60:61]\n\t"
    "v_pk_mul_f32 v[126:127], v[126:127], v[62:63]\n\t"
    "v_pk_mul_f32 v[128:129], v[128:129], v[64:65]\n\t"
    "v_pk_mul_f32 v[130:131], v[130:131], v[66:67]\n\t"
    "v_pk_mul_f32 v[68:69], v[124:125], v[124:125]\n\t"
    "v_pk_mul_f32 v[70:71], v[126:127], v[126:127]\n\t"
    "v_pk_mul_f32 v[72:73], v[128:129], v[128:129]\n\t"
    "v_pk_mul_f32 v[74:75], v[130:131], v[130:131]\n\t"
    "v_xor_b32 v102, s63, v100\n\t"
    "v_xor_b32 v103, s63, v101\n\t"
    "v_pk_fma_f32 v[60:61], v[172:173], v[172:173], s[50:51] neg_lo:[0,1,0] neg_hi:[0,1,0]\n\t"
    "v_pk_fma_f32 v[62:63], v[174:175], v[174:175], s[50:51] neg_lo:[0,1,0] neg_hi:[0,1,0]\n\t"
    "v_pk_fma_f32 v[64:65], v[176:177], v[176:177], s[50:51] neg_lo:[0,1,0] neg_hi:[0,1,0]\n\t"
    "v_pk_fma_f32 v[66:67], v[178:179], v[178:179], s[50:51] neg_lo:[0,1,0] neg_hi:[0,1,0]\n\t"
    "v_pk_mul_f32 v[60:61], v[60:61], v[172:173]\n\t"
    "v_pk_mul_f32 v[62:63], v[62:63], v[174:175]\n\t"
    "v_pk_mul_f32 v[64:65], v[64:65], v[176:177]\n\t"
    "v_pk_mul_f32 v[66:67], v[66:67], v[178:179]\n\t"
    "v_mfma_f32_16x16x32_bf16 v[220:223], v[36:39], v[24:27], 0\n\t"
    "ds_read_b64_tr_b16 v[36:37], v102\n\t"
    "ds_read_b64_tr_b16 v[38:39], v102 offset:2048\n\t"
    "v_exp_f32 v60, v60\n\t"
    "v_exp_f32 v61, v61\n\t"
    "v_mfma_f32_16x16x32_bf16 v[224:227], v[48:51], v[24:27], 0\n\t"
    "ds_read_b64_tr_b16 v[48:49], v103\n\t"
    "ds_read_b64_tr_b16 v[50:51], v103 offset:2048\n\t"
    "v_exp_f32 v62, v62\n\t"
    "v_exp_f32 v63, v63\n\t"
    "v_mfma_f32_16x16x32_bf16 v[220:223], v[40:43], v[28:31], v[220:223]\n\t"
    "ds_read_b64_tr_b16 v[40:41], v102 offset:4096\n\t"
    "ds_read_b64_tr_b16 v[42:43], v102 offset:6144\n\t"
    "v_exp_f32 v64, v64\n\t"
    "v_exp_f32 v65, v65\n\t"
    "v_exp_f32 v66, v66\n\t"
    "v_exp_f32 v67, v67\n\t"
    "v_pk_add_f32 v[60:61], v[60:61], 1.0 op_sel_hi:[1,0]\n\t"
    "v_pk_add_f32 v[62:63], v[62:63], 1.0 op_sel_hi:[1,0]\n\t"
    "v_pk_add_f32 v[64:65], v[64:65], 1.0 op_sel_hi:[1,0]\n\t"
    "v_pk_add_f32 v[66:67], v[66:67], 1.0 op_sel_hi:[1,0]\n\t"
    "v_mfma_f32_16x16x32_bf16 v[224:227], v[52:55], v[28:31], v[224:227]\n\t"
    "ds_read_b64_tr_b16 v[52:53], v103 offset:4096\n\t"
    "ds_read_b64_tr_b16 v[54:55], v103 offset:6144\n\t"
    "v_rcp_f32 v60, v60\n\t"
    "v_rcp_f32 v61, v61\n\t"
    "v_mfma_f32_16x16x32_bf16 v[220:223], v[44:47], v[32:35], v[220:223]\n\t"
    "ds_read_b64_tr_b16 v[44:45], v102 offset:8192\n\t"
    "ds_read_b64_tr_b16 v[46:47], v102 offset:10240\n\t"
    "v_rcp_f32 v62, v62\n\t"
    "v_rcp_f32 v63, v63\n\t"
    "v_mfma_f32_16x16x32_bf16 v[224:227], v[56:59], v[32:35], v[224:227]\n\t"
    "ds_read_b64_tr_b16 v[56:57], v103 offset:8192\n\t"
    "ds_read_b64_tr_b16 v[58:59], v103 offset:10240\n\t"
    "v_rcp_f32 v64, v64\n\t"
    "v_rcp_f32 v65, v65\n\t"
    "s_add_u32 m0, s53, 5120\n\t"
    "s_add_u32 s78, s53, 3072\n\t"
    "buffer_load_dwordx4 v246, s[12:15], s78 offen lds\n\t"
    "v_rcp_f32 v66, v66\n\t"
    "v_rcp_f32 v67, v67\n\t"
    "v_pk_mul_f32 v[172:173], v[172:173], v[60:61]\n\t"
    "v_pk_mul_f32 v[174:175], v[174:175], v[62:63]\n\t"
    "v_pk_mul_f32 v[176:177], v[176:177], v[64:65]\n\t"
    "v_pk_mul_f32 v[178:179], v[178:179], v[66:67]\n\t"
    "v_pk_fma_f32 v[68:69], v[172:173], v[172:173], v[68:69]\n\t"
    "v_pk_fma_f32 v[70:71], v[174:175], v[174:175], v[70:71]\n\t"
    "v_pk_fma_f32 v[72:73], v[176:177], v[176:177], v[72:73]\n\t"
    "v_pk_fma_f32 v[74:75], v[178:179], v[178:179], v[74:75]\n\t"
    "v_pk_fma_f32 v[60:61], v[220:221], v[220:221], s[50:51] neg_lo:[0,1,0] neg_hi:[0,1,0]\n\t"
    "v_pk_fma_f32 v[62:63], v[222:223], v[222:223], s[50:51] neg_lo:[0,1,0] neg_hi:[0,1,0]\n\t"
    "v_pk_fma_f32 v[64:65], v[224:225], v[224:225], s[50:51] neg_lo:[0,1,0] neg_hi:[0,1,0]\n\t"
    "v_pk_fma_f32 v[66:67], v[226:227], v[226:227], s[50:51] neg_lo:[0,1,0] neg_hi:[0,1,0]\n\t"
    "v_pk_mul_f32 v[60:61], v[60:61], v[220:221]\n\t"
    "v_pk_mul_f32 v[62:63], v[62:63], v[222:223]\n\t"
    "v_pk_mul_f32 v[64:65], v[64:65], v[224:225]\n\t"
    "v_pk_mul_f32 v[66:67], v[66:67], v[226:227]\n\t"
    "s_waitcnt lgkmcnt(10)\n\t"
    "v_mfma_f32_16x16x32_bf16 v[132:135], v[36:39], v[88:91], 0\n\t"
    "v_exp_f32 v60, v60\n\t"
    "v_exp_f32 v61, v61\n\t"
    "s_waitcnt lgkmcnt(8)\n\t"
    "v_mfma_f32_16x16x32_bf16 v[136:139], v[48:51], v[88:91], 0\n\t"
    "v_exp_f32 v62, v62\n\t"
    "v_exp_f32 v63, v63\n\t"
    "s_waitcnt lgkmcnt(6)\n\t"
    "v_mfma_f32_16x16x32_bf16 v[132:135], v[40:43], v[92:95], v[132:135]\n\t"
    "v_exp_f32 v64, v64\n\t"
    "v_exp_f32 v65, v65\n\t"
    "v_exp_f32 v66, v66\n\t"
    "v_exp_f32 v67, v67\n\t"
    "v_pk_add_f32 v[60:61], v[60:61], 1.0 op_sel_hi:[1,0]\n\t"
    "v_pk_add_f32 v[62:63], v[62:63], 1.0 op_sel_hi:[1,0]\n\t"
    "v_pk_add_f32 v[64:65], v[64:65], 1.0 op_sel_hi:[1,0]\n\t"
    "v_pk_add_f32 v[66:67], v[66:67], 1.0 op_sel_hi:[1,0]\n\t"
    "s_waitcnt lgkmcnt(4)\n\t"
    "v_mfma_f32_16x16x32_bf16 v[136:139], v[52:55], v[92:95], v[136:139]\n\t"
    "v_rcp_f32 v60, v60\n\t"
    "v_rcp_f32 v61, v61\n\t"
    "s_waitcnt lgkmcnt(2)\n\t"
    "v_mfma_f32_16x16x32_bf16 v[132:135], v[44:47], v[96:99], v[132:135]\n\t"
    "v_rcp_f32 v62, v62\n\t"
    "v_rcp_f32 v63, v63\n\t"
    "s_waitcnt lgkmcnt(0)\n\t"
    "v_mfma_f32_16x16x32_bf16 v[136:139], v[56:59], v[96:99], v[136:139]\n\t"
    "v_rcp_f32 v64, v64\n\t"
    "v_rcp_f32 v65, v65\n\t"
    "s_add_u32 m0, s53, 6144\n\t"
    "s_add_u32 s78, s53, 6144\n\t"
    "buffer_load_dwordx4 v244, s[12:15], s78 offen lds\n\t"
    "v_rcp_f32 v66, v66\n\t"
    "v_rcp_f32 v67, v67\n\t"
    "v_pk_mul_f32 v[220:221], v[220:221], v[60:61]\n\t"
    "v_pk_mul_f32 v[222:223], v[222:223], v[62:63]\n\t"
    "v_pk_mul_f32 v[224:225], v[224:225], v[64:65]\n\t"
    "v_pk_mul_f32 v[226:227], v[226:227], v[66:67]\n\t"
    "v_pk_fma_f32 v[68:69], v[220:221], v[220:221], v[68:69]\n\t"
    "v_pk_fma_f32 v[70:71], v[222:223], v[222:223], v[70:71]\n\t"
    "v_pk_fma_f32 v[72:73], v[224:225], v[224:225], v[72:73]\n\t"
    "v_pk_fma_f32 v[74:75], v[226:227], v[226:227], v[74:75]\n\t"
    "v_cvt_pk_bf16_f32 v84, v68, v69\n\t"
    "v_cvt_pk_bf16_f32 v85, v70, v71\n\t"
    "v_cvt_pk_bf16_f32 v86, v72, v73\n\t"
    "v_cvt_pk_bf16_f32 v87, v74, v75\n\t"
    "ds_write_b128 v104, v[84:87]\n\t"
    "v_xor_b32 v4, 128, v105\n\t"
    "ds_read_b64_tr_b16 v[0:1], v105\n\t"
    "ds_read_b64_tr_b16 v[2:3], v4\n\t"
    "s_setprio 2\n\t"
    "v_pk_fma_f32 v[60:61], v[132:133], v[132:133], s[50:51] neg_lo:[0,1,0] neg_hi:[0,1,0]\n\t"
    "v_pk_fma_f32 v[62:63], v[134:135], v[134:135], s[50:51] neg_lo:[0,1,0] neg_hi:[0,1,0]\n\t"
    "v_pk_fma_f32 v[64:65], v[136:137], v[136:137], s[50:51] neg_lo:[0,1,0] neg_hi:[0,1,0]\n\t"
    "v_pk_fma_f32 v[66:67], v[138:139], v[138:139], s[50:51] neg_lo:[0,1,0] neg_hi:[0,1,0]\n\t"
    "v_pk_mul_f32 v[60:61], v[60:61], v[132:133]\n\t"
    "v_pk_mul_f32 v[62:63], v[62:63], v[134:135]\n\t"
    "v_pk_mul_f32 v[64:65], v[64:65], v[136:137]\n\t"
    "v_pk_mul_f32 v[66:67], v[66:67], v[138:139]\n\t"
    "v_mfma_f32_16x16x32_bf16 v[180:183], v[36:39], v[12:15], 0\n\t"
    "v_exp_f32 v60, v60\n\t"
    "v_exp_f32 v61, v61\n\t"
    "v_mfma_f32_16x16x32_bf16 v[184:187], v[48:51], v[12:15], 0\n\t"
    "v_exp_f32 v62, v62\n\t"
    "v_exp_f32 v63, v63\n\t"
    "v_mfma_f32_16x16x32_bf16 v[180:183], v[40:43], v[16:19], v[180:183]\n\t"
    "v_exp_f32 v64, v64\n\t"
    "v_exp_f32 v65, v65\n\t"
    "s_waitcnt lgkmcnt(0)\n\t"
    "v_mfma_f32_16x16x32_bf16 v[4:7], v[8:11], v[0:3], 0\n\t"
    "v_exp_f32 v66, v66\n\t"
    "v_exp_f32 v67, v67\n\t"
    "v_pk_add_f32 v[60:61], v[60:61], 1.0 op_sel_hi:[1,0]\n\t"
    "v_pk_add_f32 v[62:63], v[62:63], 1.0 op_sel_hi:[1,0]\n\t"
    "v_pk_add_f32 v[64:65], v[64:65], 1.0 op_sel_hi:[1,0]\n\t"
    "v_pk_add_f32 v[66:67], v[66:67], 1.0 op_sel_hi:[1,0]\n\t"
    "v_mfma_f32_16x16x32_bf16 v[184:187], v[52:55], v[16:19], v[184:187]\n\t"
    "v_rcp_f32 v60, v60\n\t"
    "v_rcp_f32 v61, v61\n\t"
    "v_mfma_f32_16x16x32_bf16 v[180:183], v[44:47], v[20:23], v[180:183]\n\t"
    "v_rcp_f32 v62, v62\n\t"
    "v_rcp_f32 v63, v63\n\t"
    "v_mfma_f32_16x16x32_bf16 v[184:187], v[56:59], v[20:23], v[184:187]\n\t"
    "v_rcp_f32 v64, v64\n\t"
    "v_rcp_f32 v65, v65\n\t"
    "s_add_u32 m0, s53, 7168\n\t"
    "s_add_u32 s78, s53, 6144\n\t"
    "buffer_load_dwordx4 v245, s[12:15], s78 offen lds\n\t"
    "v_rcp_f32 v66, v66\n\t"
    "v_rcp_f32 v67, v67\n\t"
    "v_cvt_pk_bf16_f32 v4, v4, v4\n\t"
    "v_lshl_add_u32 v5, s68, 8, v107\n\t"
    "ds_write_b16 v5, v4\n\t"
    "v_pk_mul_f32 v[132:133], v[132:133], v[60:61]\n\t"
    "v_pk_mul_f32 v[134:135], v[134:135], v[62:63]\n\t"
    "v_pk_mul_f32 v[136:137], v[136:137], v[64:65]\n\t"
    "v_pk_mul_f32 v[138:139], v[138:139], v[66:67]\n\t"
    "v_pk_mul_f32 v[68:69], v[132:133], v[132:133]\n\t"
    "v_pk_mul_f32 v[70:71], v[134:135], v[134:135]\n\t"
    "v_pk_mul_f32 v[72:73], v[136:137], v[136:137]\n\t"
    "v_pk_mul_f32 v[74:75], v[138:139], v[138:139]\n\t"
    "v_xor_b32 v102, s64, v100\n\t"
    "v_xor_b32 v103, s64, v101\n\t"
    "v_pk_fma_f32 v[60:61], v[180:181], v[180:181], s[50:51] neg_lo:[0,1,0] neg_hi:[0,1,0]\n\t"
    "v_pk_fma_f32 v[62:63], v[182:183], v[182:183], s[50:51] neg_lo:[0,1,0] neg_hi:[0,1,0]\n\t"
    "v_pk_fma_f32 v[64:65], v[184:185], v[184:185], s[50:51] neg_lo:[0,1,0] neg_hi:[0,1,0]\n\t"
    "v_pk_fma_f32 v[66:67], v[186:187], v[186:187], s[50:51] neg_lo:[0,1,0] neg_hi:[0,1,0]\n\t"
    "v_pk_mul_f32 v[60:61], v[60:61], v[180:181]\n\t"
    "v_pk_mul_f32 v[62:63], v[62:63], v[182:183]\n\t"
    "v_pk_mul_f32 v[64:65], v[64:65], v[184:185]\n\t"
    "v_pk_mul_f32 v[66:67], v[66:67], v[186:187]\n\t"
    "v_mfma_f32_16x16x32_bf16 v[228:231], v[36:39], v[24:27], 0\n\t"
    "ds_read_b64_tr_b16 v[36:37], v102\n\t"
    "ds_read_b64_tr_b16 v[38:39], v102 offset:2048\n\t"
    "v_exp_f32 v60, v60\n\t"
    "v_exp_f32 v61, v61\n\t"
    "v_mfma_f32_16x16x32_bf16 v[232:235], v[48:51], v[24:27], 0\n\t"
    "ds_read_b64_tr_b16 v[48:49], v103\n\t"
    "ds_read_b64_tr_b16 v[50:51], v103 offset:2048\n\t"
    "v_exp_f32 v62, v62\n\t"
    "v_exp_f32 v63, v63\n\t"
    "v_mfma_f32_16x16x32_bf16 v[228:231], v[40:43], v[28:31], v[228:231]\n\t"
    "ds_read_b64_tr_b16 v[40:41], v102 offset:4096\n\t"
    "ds_read_b64_tr_b16 v[42:43], v102 offset:6144\n\t"
    "v_exp_f32 v64, v64\n\t"
    "v_exp_f32 v65, v65\n\t"
    "v_exp_f32 v66, v66\n\t"
    "v_exp_f32 v67, v67\n\t"
    "v_pk_add_f32 v[60:61], v[60:61], 1.0 op_sel_hi:[1,0]\n\t"
    "v_pk_add_f32 v[62:63], v[62:63], 1.0 op_sel_hi:[1,0]\n\t"
    "v_pk_add_f32 v[64:65], v[64:65], 1.0 op_sel_hi:[1,0]\n\t"
    "v_pk_add_f32 v[66:67], v[66:67], 1.0 op_sel_hi:[1,0]\n\t"
    "v_mfma_f32_16x16x32_bf16 v[232:235], v[52:55], v[28:31], v[232:235]\n\t"
    "ds_read_b64_tr_b16 v[52:53], v103 offset:4096\n\t"
    "ds_read_b64_tr_b16 v[54:55], v103 offset:6144\n\t"
    "v_rcp_f32 v60, v60\n\t"
    "v_rcp_f32 v61, v61\n\t"
    "v_mfma_f32_16x16x32_bf16 v[228:231], v[44:47], v[32:35], v[228:231]\n\t"
    "ds_read_b64_tr_b16 v[44:45], v102 offset:8192\n\t"
    "ds_read_b64_tr_b16 v[46:47], v102 offset:10240\n\t"
    "v_rcp_f32 v62, v62\n\t"
    "v_rcp_f32 v63, v63\n\t"
    "v_mfma_f32_16x16x32_bf16 v[232:235], v[56:59], v[32:35], v[232:235]\n\t"
    "ds_read_b64_tr_b16 v[56:57], v103 offset:8192\n\t"
    "ds_read_b64_tr_b16 v[58:59], v103 offset:10240\n\t"
    "v_rcp_f32 v64, v64\n\t"
    "v_rcp_f32 v65, v65\n\t"
    "s_add_u32 m0, s53, 8192\n\t"
    "s_add_u32 s78, s53, 6144\n\t"
    "buffer_load_dwordx4 v246, s[12:15], s78 offen lds\n\t"
    "v_rcp_f32 v66, v66\n\t"
    "v_rcp_f32 v67, v67\n\t"
    "v_pk_mul_f32 v[180:181], v[180:181], v[60:61]\n\t"
    "v_pk_mul_f32 v[182:183], v[182:183], v[62:63]\n\t"
    "v_pk_mul_f32 v[184:185], v[184:185], v[64:65]\n\t"
    "v_pk_mul_f32 v[186:187], v[186:187], v[66:67]\n\t"
    "v_pk_fma_f32 v[68:69], v[180:181], v[180:181], v[68:69]\n\t"
    "v_pk_fma_f32 v[70:71], v[182:183], v[182:183], v[70:71]\n\t"
    "v_pk_fma_f32 v[72:73], v[184:185], v[184:185], v[72:73]\n\t"
    "v_pk_fma_f32 v[74:75], v[186:187], v[186:187], v[74:75]\n\t"
    "v_pk_fma_f32 v[60:61], v[228:229], v[228:229], s[50:51] neg_lo:[0,1,0] neg_hi:[0,1,0]\n\t"
    "v_pk_fma_f32 v[62:63], v[230:231], v[230:231], s[50:51] neg_lo:[0,1,0] neg_hi:[0,1,0]\n\t"
    "v_pk_fma_f32 v[64:65], v[232:233], v[232:233], s[50:51] neg_lo:[0,1,0] neg_hi:[0,1,0]\n\t"
    "v_pk_fma_f32 v[66:67], v[234:235], v[234:235], s[50:51] neg_lo:[0,1,0] neg_hi:[0,1,0]\n\t"
    "v_pk_mul_f32 v[60:61], v[60:61], v[228:229]\n\t"
    "v_pk_mul_f32 v[62:63], v[62:63], v[230:231]\n\t"
    "v_pk_mul_f32 v[64:65], v[64:65], v[232:233]\n\t"
    "v_pk_mul_f32 v[66:67], v[66:67], v[234:235]\n\t"
    "s_waitcnt lgkmcnt(10)\n\t"
    "v_mfma_f32_16x16x32_bf16 v[140:143], v[36:39], v[88:91], 0\n\t"
    "v_exp_f32 v60, v60\n\t"
    "v_exp_f32 v61, v61\n\t"
    "s_waitcnt lgkmcnt(8)\n\t"
    "v_mfma_f32_16x16x32_bf16 v[144:147], v[48:51], v[88:91], 0\n\t"
    "v_exp_f32 v62, v62\n\t"
    "v_exp_f32 v63, v63\n\t"
    "s_waitcnt lgkmcnt(6)\n\t"
    "v_mfma_f32_16x16x32_bf16 v[140:143], v[40:43], v[92:95], v[140:143]\n\t"
    "v_exp_f32 v64, v64\n\t"
    "v_exp_f32 v65, v65\n\t"
    "v_exp_f32 v66, v66\n\t"
    "v_exp_f32 v67, v67\n\t"
    "v_pk_add_f32 v[60:61], v[60:61], 1.0 op_sel_hi:[1,0]\n\t"
    "v_pk_add_f32 v[62:63], v[62:63], 1.0 op_sel_hi:[1,0]\n\t"
    "v_pk_add_f32 v[64:65], v[64:65], 1.0 op_sel_hi:[1,0]\n\t"
    "v_pk_add_f32 v[66:67], v[66:67], 1.0 op_sel_hi:[1,0]\n\t"
    "s_waitcnt lgkmcnt(4)\n\t"
    "v_mfma_f32_16x16x32_bf16 v[144:147], v[52:55], v[92:95], v[144:147]\n\t"
    "v_rcp_f32 v60, v60\n\t"
    "v_rcp_f32 v61, v61\n\t"
    "s_waitcnt lgkmcnt(2)\n\t"
    "v_mfma_f32_16x16x32_bf16 v[140:143], v[44:47], v[96:99], v[140:143]\n\t"
    "v_rcp_f32 v62, v62\n\t"
    "v_rcp_f32 v63, v63\n\t"
    "s_waitcnt lgkmcnt(0)\n\t"
    "v_mfma_f32_16x16x32_bf16 v[144:147], v[56:59], v[96:99], v[144:147]\n\t"
    "v_rcp_f32 v64, v64\n\t"
    "v_rcp_f32 v65, v65\n\t"
    "v_rcp_f32 v66, v66\n\t"
    "v_rcp_f32 v67, v67\n\t"
    "v_pk_mul_f32 v[228:229], v[228:229], v[60:61]\n\t"
    "v_pk_mul_f32 v[230:231], v[230:231], v[62:63]\n\t"
    "v_pk_mul_f32 v[232:233], v[232:233], v[64:65]\n\t"
    "v_pk_mul_f32 v[234:235], v[234:235], v[66:67]\n\t"
    "v_pk_fma_f32 v[68:69], v[228:229], v[228:229], v[68:69]\n\t"
    "v_pk_fma_f32 v[70:71], v[230:231], v[230:231], v[70:71]\n\t"
    "v_pk_fma_f32 v[72:73], v[232:233], v[232:233], v[72:73]\n\t"
    "v_pk_fma_f32 v[74:75], v[234:235], v[234:235], v[74:75]\n\t"
    "v_cvt_pk_bf16_f32 v84, v68, v69\n\t"
    "v_cvt_pk_bf16_f32 v85, v70, v71\n\t"
    "v_cvt_pk_bf16_f32 v86, v72, v73\n\t"
    "v_cvt_pk_bf16_f32 v87, v74, v75\n\t"
    "ds_write_b128 v104, v[84:87]\n\t"
    "v_xor_b32 v4, 128, v105\n\t"
    "ds_read_b64_tr_b16 v[0:1], v105\n\t"
    "ds_read_b64_tr_b16 v[2:3], v4\n\t"
    "v_pk_fma_f32 v[60:61], v[140:141], v[140:141], s[50:51] neg_lo:[0,1,0] neg_hi:[0,1,0]\n\t"
    "v_pk_fma_f32 v[62:63], v[142:143], v[142:143], s[50:51] neg_lo:[0,1,0] neg_hi:[0,1,0]\n\t"
    "v_pk_fma_f32 v[64:65], v[144:145], v[144:145], s[50:51] neg_lo:[0,1,0] neg_hi:[0,1,0]\n\t"
    "v_pk_fma_f32 v[66:67], v[146:147], v[146:147], s[50:51] neg_lo:[0,1,0] neg_hi:[0,1,0]\n\t"
    "v_pk_mul_f32 v[60:61], v[60:61], v[140:141]\n\t"
    "v_pk_mul_f32 v[62:63], v[62:63], v[142:143]\n\t"
    "v_pk_mul_f32 v[64:65], v[64:65], v[144:145]\n\t"
    "v_pk_mul_f32 v[66:67], v[66:67], v[146:147]\n\t"
    "v_mfma_f32_16x16x32_bf16 v[188:191], v[36:39], v[12:15], 0\n\t"
    "v_exp_f32 v60, v60\n\t"
    "v_exp_f32 v61, v61\n\t"
    "v_mfma_f32_16x16x32_bf16 v[192:195], v[48:51], v[12:15], 0\n\t"
    "v_exp_f32 v62, v62\n\t"
    "v_exp_f32 v63, v63\n\t"
    "v_mfma_f32_16x16x32_bf16 v[188:191], v[40:43], v[16:19], v[188:191]\n\t"
    "v_exp_f32 v64, v64\n\t"
    "v_exp_f32 v65, v65\n\t"
    "s_waitcnt lgkmcnt(0)\n\t"
    "v_mfma_f32_16x16x32_bf16 v[4:7], v[8:11], v[0:3], 0\n\t"
    "v_exp_f32 v66, v66\n\t"
    "v_exp_f32 v67, v67\n\t"
    "v_pk_add_f32 v[60:61], v[60:61], 1.0 op_sel_hi:[1,0]\n\t"
    "v_pk_add_f32 v[62:63], v[62:63], 1.0 op_sel_hi:[1,0]\n\t"
    "v_pk_add_f32 v[64:65], v[64:65], 1.0 op_sel_hi:[1,0]\n\t"
    "v_pk_add_f32 v[66:67], v[66:67], 1.0 op_sel_hi:[1,0]\n\t"
    "v_mfma_f32_16x16x32_bf16 v[192:195], v[52:55], v[16:19], v[192:195]\n\t"
    "v_rcp_f32 v60, v60\n\t"
    "v_rcp_f32 v61, v61\n\t"
    "v_mfma_f32_16x16x32_bf16 v[188:191], v[44:47], v[20:23], v[188:191]\n\t"
    "v_rcp_f32 v62, v62\n\t"
    "v_rcp_f32 v63, v63\n\t"
    "v_mfma_f32_16x16x32_bf16 v[192:195], v[56:59], v[20:23], v[192:195]\n\t"
    "v_rcp_f32 v64, v64\n\t"
    "v_rcp_f32 v65, v65\n\t"
    "v_rcp_f32 v66, v66\n\t"
    "v_rcp_f32 v67, v67\n\t"
    "v_cvt_pk_bf16_f32 v4, v4, v4\n\t"
    "v_lshl_add_u32 v5, s69, 8, v107\n\t"
    "ds_write_b16 v5, v4\n\t"
    "v_pk_mul_f32 v[140:141], v[140:141], v[60:61]\n\t"
    "v_pk_mul_f32 v[142:143], v[142:143], v[62:63]\n\t"
    "v_pk_mul_f32 v[144:145], v[144:145], v[64:65]\n\t"
    "v_pk_mul_f32 v[146:147], v[146:147], v[66:67]\n\t"
    "v_pk_mul_f32 v[68:69], v[140:141], v[140:141]\n\t"
    "v_pk_mul_f32 v[70:71], v[142:143], v[142:143]\n\t"
    "v_pk_mul_f32 v[72:73], v[144:145], v[144:145]\n\t"
    "v_pk_mul_f32 v[74:75], v[146:147], v[146:147]\n\t"
    "v_xor_b32 v102, s65, v100\n\t"
    "v_xor_b32 v103, s65, v101\n\t"
    "v_pk_fma_f32 v[60:61], v[188:189], v[188:189], s[50:51] neg_lo:[0,1,0] neg_hi:[0,1,0]\n\t"
    "v_pk_fma_f32 v[62:63], v[190:191], v[190:191], s[50:51] neg_lo:[0,1,0] neg_hi:[0,1,0]\n\t"
    "v_pk_fma_f32 v[64:65], v[192:193], v[192:193], s[50:51] neg_lo:[0,1,0] neg_hi:[0,1,0]\n\t"
    "v_pk_fma_f32 v[66:67], v[194:195], v[194:195], s[50:51] neg_lo:[0,1,0] neg_hi:[0,1,0]\n\t"
    "v_pk_mul_f32 v[60:61], v[60:61], v[188:189]\n\t"
    "v_pk_mul_f32 v[62:63], v[62:63], v[190:191]\n\t"
    "v_pk_mul_f32 v[64:65], v[64:65], v[192:193]\n\t"
    "v_pk_mul_f32 v[66:67], v[66:67], v[194:195]\n\t"
    "v_mfma_f32_16x16x32_bf16 v[236:239], v[36:39], v[24:27], 0\n\t"
    "ds_read_b64_tr_b16 v[36:37], v102\n\t"
    "ds_read_b64_tr_b16 v[38:39], v102 offset:2048\n\t"
    "v_exp_f32 v60, v60\n\t"
    "v_exp_f32 v61, v61\n\t"
    "v_mfma_f32_16x16x32_bf16 v[240:243], v[48:51], v[24:27], 0\n\t"
    "ds_read_b64_tr_b16 v[48:49], v103\n\t"
    "ds_read_b64_tr_b16 v[50:51], v103 offset:2048\n\t"
    "v_exp_f32 v62, v62\n\t"
    "v_exp_f32 v63, v63\n\t"
    "v_mfma_f32_16x16x32_bf16 v[236:239], v[40:43], v[28:31], v[236:239]\n\t"
    "ds_read_b64_tr_b16 v[40:41], v102 offset:4096\n\t"
    "ds_read_b64_tr_b16 v[42:43], v102 offset:6144\n\t"
    "v_exp_f32 v64, v64\n\t"
    "v_exp_f32 v65, v65\n\t"
    "v_exp_f32 v66, v66\n\t"
    "v_exp_f32 v67, v67\n\t"
    "v_pk_add_f32 v[60:61], v[60:61], 1.0 op_sel_hi:[1,0]\n\t"
    "v_pk_add_f32 v[62:63], v[62:63], 1.0 op_sel_hi:[1,0]\n\t"
    "v_pk_add_f32 v[64:65], v[64:65], 1.0 op_sel_hi:[1,0]\n\t"
    "v_pk_add_f32 v[66:67], v[66:67], 1.0 op_sel_hi:[1,0]\n\t"
    "v_mfma_f32_16x16x32_bf16 v[240:243], v[52:55], v[28:31], v[240:243]\n\t"
    "ds_read_b64_tr_b16 v[52:53], v103 offset:4096\n\t"
    "ds_read_b64_tr_b16 v[54:55], v103 offset:6144\n\t"
    "v_rcp_f32 v60, v60\n\t"
    "v_rcp_f32 v61, v61\n\t"
    "v_mfma_f32_16x16x32_bf16 v[236:239], v[44:47], v[32:35], v[236:239]\n\t"
    "ds_read_b64_tr_b16 v[44:45], v102 offset:8192\n\t"
    "ds_read_b64_tr_b16 v[46:47], v102 offset:10240\n\t"
    "v_rcp_f32 v62, v62\n\t"
    "v_rcp_f32 v63, v63\n\t"
    "v_mfma_f32_16x16x32_bf16 v[240:243], v[56:59], v[32:35], v[240:243]\n\t"
    "ds_read_b64_tr_b16 v[56:57], v103 offset:8192\n\t"
    "ds_read_b64_tr_b16 v[58:59], v103 offset:10240\n\t"
    "v_rcp_f32 v64, v64\n\t"
    "v_rcp_f32 v65, v65\n\t"
    "v_rcp_f32 v66, v66\n\t"
    "v_rcp_f32 v67, v67\n\t"
    "v_pk_mul_f32 v[188:189], v[188:189], v[60:61]\n\t"
    "v_pk_mul_f32 v[190:191], v[190:191], v[62:63]\n\t"
    "v_pk_mul_f32 v[192:193], v[192:193], v[64:65]\n\t"
    "v_pk_mul_f32 v[194:195], v[194:195], v[66:67]\n\t"
    "v_pk_fma_f32 v[68:69], v[188:189], v[188:189], v[68:69]\n\t"
    "v_pk_fma_f32 v[70:71], v[190:191], v[190:191], v[70:71]\n\t"
    "v_pk_fma_f32 v[72:73], v[192:193], v[192:193], v[72:73]\n\t"
    "v_pk_fma_f32 v[74:75], v[194:195], v[194:195], v[74:75]\n\t"
    "s_waitcnt vmcnt(9)\n\t"
    "v_pk_fma_f32 v[60:61], v[236:237], v[236:237], s[50:51] neg_lo:[0,1,0] neg_hi:[0,1,0]\n\t"
    "v_pk_fma_f32 v[62:63], v[238:239], v[238:239], s[50:51] neg_lo:[0,1,0] neg_hi:[0,1,0]\n\t"
    "v_pk_fma_f32 v[64:65], v[240:241], v[240:241], s[50:51] neg_lo:[0,1,0] neg_hi:[0,1,0]\n\t"
    "v_pk_fma_f32 v[66:67], v[242:243], v[242:243], s[50:51] neg_lo:[0,1,0] neg_hi:[0,1,0]\n\t"
    "v_pk_mul_f32 v[60:61], v[60:61], v[236:237]\n\t"
    "v_pk_mul_f32 v[62:63], v[62:63], v[238:239]\n\t"
    "v_pk_mul_f32 v[64:65], v[64:65], v[240:241]\n\t"
    "v_pk_mul_f32 v[66:67], v[66:67], v[242:243]\n\t"
    "s_waitcnt lgkmcnt(10)\n\t"
    "v_mfma_f32_16x16x32_bf16 v[148:151], v[36:39], v[88:91], 0\n\t"
    "v_exp_f32 v60, v60\n\t"
    "v_exp_f32 v61, v61\n\t"
    "s_waitcnt lgkmcnt(8)\n\t"
    "v_mfma_f32_16x16x32_bf16 v[152:155], v[48:51], v[88:91], 0\n\t"
    "v_exp_f32 v62, v62\n\t"
    "v_exp_f32 v63, v63\n\t"
    "s_waitcnt lgkmcnt(6)\n\t"
    "v_mfma_f32_16x16x32_bf16 v[148:151], v[40:43], v[92:95], v[148:151]\n\t"
    "v_exp_f32 v64, v64\n\t"
    "v_exp_f32 v65, v65\n\t"
    "v_exp_f32 v66, v66\n\t"
    "v_exp_f32 v67, v67\n\t"
    "v_pk_add_f32 v[60:61], v[60:61], 1.0 op_sel_hi:[1,0]\n\t"
    "v_pk_add_f32 v[62:63], v[62:63], 1.0 op_sel_hi:[1,0]\n\t"
    "v_pk_add_f32 v[64:65], v[64:65], 1.0 op_sel_hi:[1,0]\n\t"
    "v_pk_add_f32 v[66:67], v[66:67], 1.0 op_sel_hi:[1,0]\n\t"
    "s_waitcnt lgkmcnt(4)\n\t"
    "v_mfma_f32_16x16x32_bf16 v[152:155], v[52:55], v[92:95], v[152:155]\n\t"
    "v_rcp_f32 v60, v60\n\t"
    "v_rcp_f32 v61, v61\n\t"
    "s_waitcnt lgkmcnt(2)\n\t"
    "v_mfma_f32_16x16x32_bf16 v[148:151], v[44:47], v[96:99], v[148:151]\n\t"
    "v_rcp_f32 v62, v62\n\t"
    "v_rcp_f32 v63, v63\n\t"
    "s_waitcnt lgkmcnt(0)\n\t"
    "v_mfma_f32_16x16x32_bf16 v[152:155], v[56:59], v[96:99], v[152:155]\n\t"
    "v_rcp_f32 v64, v64\n\t"
    "v_rcp_f32 v65, v65\n\t"
    "v_rcp_f32 v66, v66\n\t"
    "v_rcp_f32 v67, v67\n\t"
    "v_pk_mul_f32 v[236:237], v[236:237], v[60:61]\n\t"
    "v_pk_mul_f32 v[238:239], v[238:239], v[62:63]\n\t"
    "v_pk_mul_f32 v[240:241], v[240:241], v[64:65]\n\t"
    "v_pk_mul_f32 v[242:243], v[242:243], v[66:67]\n\t"
    "v_pk_fma_f32 v[68:69], v[236:237], v[236:237], v[68:69]\n\t"
    "v_pk_fma_f32 v[70:71], v[238:239], v[238:239], v[70:71]\n\t"
    "v_pk_fma_f32 v[72:73], v[240:241], v[240:241], v[72:73]\n\t"
    "v_pk_fma_f32 v[74:75], v[242:243], v[242:243], v[74:75]\n\t"
    "v_cvt_pk_bf16_f32 v84, v68, v69\n\t"
    "v_cvt_pk_bf16_f32 v85, v70, v71\n\t"
    "v_cvt_pk_bf16_f32 v86, v72, v73\n\t"
    "v_cvt_pk_bf16_f32 v87, v74, v75\n\t"
    "ds_write_b128 v104, v[84:87]\n\t"
    "v_xor_b32 v4, 128, v105\n\t"
    "ds_read_b64_tr_b16 v[0:1], v105\n\t"
    "ds_read_b64_tr_b16 v[2:3], v4\n\t"
    "s_setprio 1\n\t"
    "v_pk_fma_f32 v[60:61], v[148:149], v[148:149], s[50:51] neg_lo:[0,1,0] neg_hi:[0,1,0]\n\t"
    "v_pk_fma_f32 v[62:63], v[150:151], v[150:151], s[50:51] neg_lo:[0,1,0] neg_hi:[0,1,0]\n\t"
    "v_pk_fma_f32 v[64:65], v[152:153], v[152:153], s[50:51] neg_lo:[0,1,0] neg_hi:[0,1,0]\n\t"
    "v_pk_fma_f32 v[66:67], v[154:155], v[154:155], s[50:51] neg_lo:[0,1,0] neg_hi:[0,1,0]\n\t"
    "v_pk_mul_f32 v[60:61], v[60:61], v[148:149]\n\t"
    "v_pk_mul_f32 v[62:63], v[62:63], v[150:151]\n\t"
    "v_pk_mul_f32 v[64:65], v[64:65], v[152:153]\n\t"
    "v_pk_mul_f32 v[66:67], v[66:67], v[154:155]\n\t"
    "v_mfma_f32_16x16x32_bf16 v[196:199], v[36:39], v[12:15], 0\n\t"
    "v_exp_f32 v60, v60\n\t"
    "v_exp_f32 v61, v61\n\t"
    "v_mfma_f32_16x16x32_bf16 v[200:203], v[48:51], v[12:15], 0\n\t"
    "v_exp_f32 v62, v62\n\t"
    "v_exp_f32 v63, v63\n\t"
    "v_mfma_f32_16x16x32_bf16 v[196:199], v[40:43], v[16:19], v[196:199]\n\t"
    "v_exp_f32 v64, v64\n\t"
    "v_exp_f32 v65, v65\n\t"
    "s_waitcnt lgkmcnt(0)\n\t"
    "v_mfma_f32_16x16x32_bf16 v[4:7], v[8:11], v[0:3], 0\n\t"
    "v_exp_f32 v66, v66\n\t"
    "v_exp_f32 v67, v67\n\t"
    "v_pk_add_f32 v[60:61], v[60:61], 1.0 op_sel_hi:[1,0]\n\t"
    "v_pk_add_f32 v[62:63], v[62:63], 1.0 op_sel_hi:[1,0]\n\t"
    "v_pk_add_f32 v[64:65], v[64:65], 1.0 op_sel_hi:[1,0]\n\t"
    "v_pk_add_f32 v[66:67], v[66:67], 1.0 op_sel_hi:[1,0]\n\t"
    "v_mfma_f32_16x16x32_bf16 v[200:203], v[52:55], v[16:19], v[200:203]\n\t"
    "v_rcp_f32 v60, v60\n\t"
    "v_rcp_f32 v61, v61\n\t"
    "v_mfma_f32_16x16x32_bf16 v[196:199], v[44:47], v[20:23], v[196:199]\n\t"
    "v_rcp_f32 v62, v62\n\t"
    "v_rcp_f32 v63, v63\n\t"
    "v_mfma_f32_16x16x32_bf16 v[200:203], v[56:59], v[20:23], v[200:203]\n\t"
    "v_rcp_f32 v64, v64\n\t"
    "v_rcp_f32 v65, v65\n\t"
    "v_rcp_f32 v66, v66\n\t"
    "v_rcp_f32 v67, v67\n\t"
    "v_cvt_pk_bf16_f32 v4, v4, v4\n\t"
    "v_lshl_add_u32 v5, s70, 8, v107\n\t"
    "ds_write_b16 v5, v4\n\t"
    "v_pk_mul_f32 v[148:149], v[148:149], v[60:61]\n\t"
    "v_pk_mul_f32 v[150:151], v[150:151], v[62:63]\n\t"
    "v_pk_mul_f32 v[152:153], v[152:153], v[64:65]\n\t"
    "v_pk_mul_f32 v[154:155], v[154:155], v[66:67]\n\t"
    "v_pk_mul_f32 v[68:69], v[148:149], v[148:149]\n\t"
    "v_pk_mul_f32 v[70:71], v[150:151], v[150:151]\n\t"
    "v_pk_mul_f32 v[72:73], v[152:153], v[152:153]\n\t"
    "v_pk_mul_f32 v[74:75], v[154:155], v[154:155]\n\t"
    "v_pk_fma_f32 v[60:61], v[196:197], v[196:197], s[50:51] neg_lo:[0,1,0] neg_hi:[0,1,0]\n\t"
    "v_pk_fma_f32 v[62:63], v[198:199], v[198:199], s[50:51] neg_lo:[0,1,0] neg_hi:[0,1,0]\n\t"
    "v_pk_fma_f32 v[64:65], v[200:201], v[200:201], s[50:51] neg_lo:[0,1,0] neg_hi:[0,1,0]\n\t"
    "v_pk_fma_f32 v[66:67], v[202:203], v[202:203], s[50:51] neg_lo:[0,1,0] neg_hi:[0,1,0]\n\t"
    "v_pk_mul_f32 v[60:61], v[60:61], v[196:197]\n\t"
    "v_pk_mul_f32 v[62:63], v[62:63], v[198:199]\n\t"
    "v_pk_mul_f32 v[64:65], v[64:65], v[200:201]\n\t"
    "v_pk_mul_f32 v[66:67], v[66:67], v[202:203]\n\t"
    "v_mfma_f32_16x16x32_bf16 v[244:247], v[36:39], v[24:27], 0\n\t"
    "v_exp_f32 v60, v60\n\t"
    "v_exp_f32 v61, v61\n\t"
    "v_mfma_f32_16x16x32_bf16 v[248:251], v[48:51], v[24:27], 0\n\t"
    "v_exp_f32 v62, v62\n\t"
    "v_exp_f32 v63, v63\n\t"
    "v_mfma_f32_16x16x32_bf16 v[244:247], v[40:43], v[28:31], v[244:247]\n\t"
    "v_exp_f32 v64, v64\n\t"
    "v_exp_f32 v65, v65\n\t"
    "v_exp_f32 v66, v66\n\t"
    "v_exp_f32 v67, v67\n\t"
    "s_setprio 0\n\t"
    "v_pk_add_f32 v[60:61], v[60:61], 1.0 op_sel_hi:[1,0]\n\t"
    "v_pk_add_f32 v[62:63], v[62:63], 1.0 op_sel_hi:[1,0]\n\t"
    "v_pk_add_f32 v[64:65], v[64:65], 1.0 op_sel_hi:[1,0]\n\t"
    "v_pk_add_f32 v[66:67], v[66:67], 1.0 op_sel_hi:[1,0]\n\t"
    "v_mfma_f32_16x16x32_bf16 v[248:251], v[52:55], v[28:31], v[248:251]\n\t"
    "v_rcp_f32 v60, v60\n\t"
    "v_rcp_f32 v61, v61\n\t"
    "v_mfma_f32_16x16x32_bf16 v[244:247], v[44:47], v[32:35], v[244:247]\n\t"
    "v_rcp_f32 v62, v62\n\t"
    "v_rcp_f32 v63, v63\n\t"
    "v_mfma_f32_16x16x32_bf16 v[248:251], v[56:59], v[32:35], v[248:251]\n\t"
    "v_rcp_f32 v64, v64\n\t"
    "v_rcp_f32 v65, v65\n\t"
    "v_rcp_f32 v66, v66\n\t"
    "v_rcp_f32 v67, v67\n\t"
    "v_pk_mul_f32 v[196:197], v[196:197], v[60:61]\n\t"
    "v_pk_mul_f32 v[198:199], v[198:199], v[62:63]\n\t"
    "v_pk_mul_f32 v[200:201], v[200:201], v[64:65]\n\t"
    "v_pk_mul_f32 v[202:203], v[202:203], v[66:67]\n\t"
    "v_pk_fma_f32 v[68:69], v[196:197], v[196:197], v[68:69]\n\t"
    "v_pk_fma_f32 v[70:71], v[198:199], v[198:199], v[70:71]\n\t"
    "v_pk_fma_f32 v[72:73], v[200:201], v[200:201], v[72:73]\n\t"
    "v_pk_fma_f32 v[74:75], v[202:203], v[202:203], v[74:75]\n\t"
    "v_pk_fma_f32 v[60:61], v[244:245], v[244:245], s[50:51] neg_lo:[0,1,0] neg_hi:[0,1,0]\n\t"
    "v_pk_fma_f32 v[62:63], v[246:247], v[246:247], s[50:51] neg_lo:[0,1,0] neg_hi:[0,1,0]\n\t"
    "v_pk_fma_f32 v[64:65], v[248:249], v[248:249], s[50:51] neg_lo:[0,1,0] neg_hi:[0,1,0]\n\t"
    "v_pk_fma_f32 v[66:67], v[250:251], v[250:251], s[50:51] neg_lo:[0,1,0] neg_hi:[0,1,0]\n\t"
    "v_pk_mul_f32 v[60:61], v[60:61], v[244:245]\n\t"
    "v_pk_mul_f32 v[62:63], v[62:63], v[246:247]\n\t"
    "v_pk_mul_f32 v[64:65], v[64:65], v[248:249]\n\t"
    "v_pk_mul_f32 v[66:67], v[66:67], v[250:251]\n\t"
    "v_exp_f32 v60, v60\n\t"
    "v_exp_f32 v61, v61\n\t"
    "v_exp_f32 v62, v62\n\t"
    "v_exp_f32 v63, v63\n\t"
    "v_exp_f32 v64, v64\n\t"
    "v_exp_f32 v65, v65\n\t"
    "v_exp_f32 v66, v66\n\t"
    "v_exp_f32 v67, v67\n\t"
    "v_pk_add_f32 v[60:61], v[60:61], 1.0 op_sel_hi:[1,0]\n\t"
    "v_pk_add_f32 v[62:63], v[62:63], 1.0 op_sel_hi:[1,0]\n\t"
    "v_pk_add_f32 v[64:65], v[64:65], 1.0 op_sel_hi:[1,0]\n\t"
    "v_pk_add_f32 v[66:67], v[66:67], 1.0 op_sel_hi:[1,0]\n\t"
    "v_rcp_f32 v60, v60\n\t"
    "v_rcp_f32 v61, v61\n\t"
    "v_rcp_f32 v62, v62\n\t"
    "v_rcp_f32 v63, v63\n\t"
    "v_rcp_f32 v64, v64\n\t"
    "v_rcp_f32 v65, v65\n\t"
    "v_rcp_f32 v66, v66\n\t"
    "v_rcp_f32 v67, v67\n\t"
    "v_mbcnt_lo_u32_b32 v22, -1, 0\n\t"
    "v_mbcnt_hi_u32_b32 v22, -1, v22\n\t"
    "v_lshrrev_b32 v100, 4, v22\n\t"
    "s_lshr_b32 s78, s36, 1\n\t"
    "s_mul_i32 s78, s78, 192\n\t"
    "v_lshl_add_u32 v100, v100, 3, s78\n\t"
    "global_load_dwordx2 v[24:25], v100, s[30:31] offset:0\n\t"
    "global_load_dwordx2 v[26:27], v100, s[30:31] offset:32\n\t"
    "global_load_dwordx2 v[28:29], v100, s[30:31] offset:64\n\t"
    "global_load_dwordx2 v[30:31], v100, s[30:31] offset:96\n\t"
    "global_load_dwordx2 v[32:33], v100, s[30:31] offset:128\n\t"
    "global_load_dwordx2 v[34:35], v100, s[30:31] offset:160\n\t"
    "v_pk_mul_f32 v[244:245], v[244:245], v[60:61]\n\t"
    "v_pk_mul_f32 v[246:247], v[246:247], v[62:63]\n\t"
    "v_pk_mul_f32 v[248:249], v[248:249], v[64:65]\n\t"
    "v_pk_mul_f32 v[250:251], v[250:251], v[66:67]\n\t"
    "v_pk_fma_f32 v[68:69], v[244:245], v[244:245], v[68:69]\n\t"
    "v_pk_fma_f32 v[70:71], v[246:247], v[246:247], v[70:71]\n\t"
    "v_pk_fma_f32 v[72:73], v[248:249], v[248:249], v[72:73]\n\t"
    "v_pk_fma_f32 v[74:75], v[250:251], v[250:251], v[74:75]\n\t"
    "v_cvt_pk_bf16_f32 v84, v68, v69\n\t"
    "v_cvt_pk_bf16_f32 v85, v70, v71\n\t"
    "v_cvt_pk_bf16_f32 v86, v72, v73\n\t"
    "v_cvt_pk_bf16_f32 v87, v74, v75\n\t"
    "ds_write_b128 v104, v[84:87]\n\t"
    "v_xor_b32 v4, 128, v105\n\t"
    "ds_read_b64_tr_b16 v[0:1], v105\n\t"
    "ds_read_b64_tr_b16 v[2:3], v4\n\t"
    "v_lshlrev_b32 v23, 4, v22\n\t"
    "v_add_u32 v12, s32, v23\n\t"
    "v_lshrrev_b32 v12, 1, v12\n\t"
    "v_add_u32 v12, 81920, v12\n\t"
    "v_lshrrev_b32 v13, 3, v22\n\t"
    "v_lshlrev_b32 v13, 14, v13\n\t"
    "v_and_b32 v14, 7, v22\n\t"
    "v_lshl_add_u32 v13, v14, 3, v13\n\t"
    "s_waitcnt lgkmcnt(0)\n\t"
    "v_mfma_f32_16x16x32_bf16 v[4:7], v[8:11], v[0:3], 0\n\t"
    "s_mul_i32 s77, s42, 64\n\t"
    "s_mul_i32 s59, s36, 131072\n\t"
    "s_add_u32 s77, s77, s59\n\t"
    "s_add_u32 s77, s77, 4096\n\t"
    "v_mov_b32 v17, s40\n\t"
    "v_mov_b32 v19, s40\n\t"
    "s_nop 1\n\t"
    "v_cvt_pk_bf16_f32 v4, v4, v4\n\t"
    "v_lshl_add_u32 v5, s71, 8, v107\n\t"
    "ds_write_b16 v5, v4\n\t"
    "s_waitcnt vmcnt(6) lgkmcnt(0)\n\t"
    "s_barrier\n\t"
    "s_cmp_gt_u32 s36, 5\n\t"
    "s_cbranch_scc1 10f\n\t"
    "ds_read_b64 v[4:5], v12\n\t"
    "s_waitcnt lgkmcnt(0)\n\t"
    "v_lshlrev_b32 v8, 16, v4\n\t"
    "v_and_b32 v9, 0xffff0000, v4\n\t"
    "v_lshlrev_b32 v10, 16, v5\n\t"
    "v_and_b32 v11, 0xffff0000, v5\n\t"
    "v_pk_add_f32 v[8:9], v[8:9], v[10:11]\n\t"
    "v_add_f32 v8, v8, v9\n\t"
    "v_add_u32 v16, 0x100, v8\n\t"
    "v_and_b32 v16, 0xfffffe00, v16\n\t"
    "v_or_b32 v16, s39, v16\n\t"
    "s_nop 1\n\t"
    "v_mov_b32_dpp v18, v16 quad_perm:[1,0,3,2] row_mask:0xf bank_mask:0xf\n\t"
    "s_mov_b32 exec_lo, 0x55555555\n\t"
    "s_mov_b32 exec_hi, 0x55555555\n\t"
    "buffer_store_dwordx4 v[16:19], v13, s[16:19], s77 offen sc1\n\t"
    "s_mov_b64 exec, -1\n\t"
    "10:\n\t"
    "s_sub_u32 s55, s36, 1\n\t"
    "s_lshr_b32 s55, s55, 1\n\t"
    "s_mov_b32 s76, s42\n\t"
    "s_cmp_gt_u32 s76, 47\n\t"
    "s_cselect_b32 s55, 1, s55\n\t"
    "s_cmp_lg_u32 s55, 0\n\t"
    "s_cbranch_scc1 52f\n\t"
    "s_setprio 3\n\t"
    "52:\n\t"
    "s_mul_i32 s78, s36, 4\n\t"
    "v_bfe_u32 v0, v22, 2, 2\n\t"
    "v_add_u32 v0, s78, v0\n\t"
    "v_mul_u32_u24 v20, 37, v0\n\t"
    "v_lshrrev_b32 v20, 8, v20\n\t"
    "v_mul_u32_u24 v20, 83328, v20\n\t"
    "v_mul_u32_u24 v0, 0x180, v0\n\t"
    "v_add_u32 v20, v20, v0\n\t"
    "v_bfe_u32 v0, v22, 1, 1\n\t"
    "v_mul_u32_u24 v0, 43008, v0\n\t"
    "v_add_u32 v20, v20, v0\n\t"
    "v_and_b32 v0, 1, v22\n\t"
    "v_mul_u32_u24 v0, 0xc0, v0\n\t"
    "v_add_u32 v20, v20, v0\n\t"
    "s_and_b32 s78, s36, 3\n\t"
    "s_mul_i32 s78, s78, 4\n\t"
    "s_add_u32 s78, s78, 32\n\t"
    "v_bfe_u32 v0, v22, 2, 2\n\t"
    "v_add_u32 v0, s78, v0\n\t"
    "v_mul_u32_u24 v21, 37, v0\n\t"
    "v_lshrrev_b32 v21, 8, v21\n\t"
    "v_mul_u32_u24 v21, 83328, v21\n\t"
    "v_mul_u32_u24 v0, 0x180, v0\n\t"
    "v_add_u32 v21, v21, v0\n\t"
    "v_bfe_u32 v0, v22, 1, 1\n\t"
    "v_mul_u32_u24 v0, 43008, v0\n\t"
    "v_add_u32 v21, v21, v0\n\t"
    "v_and_b32 v0, 1, v22\n\t"
    "v_mul_u32_u24 v0, 0xc0, v0\n\t"
    "v_add_u32 v21, v21, v0\n\t"
    "v_and_b32 v19, 48, v22\n\t"
    "v_add_u32 v0, v20, v19\n\t"
    "v_add_u32 v1, v21, v19\n\t"
    "s_mul_i32 s57, s38, 128\n\t"
    "s_xor_b32 s56, s57, 128\n\t"
    "s_add_u32 s56, s56, s43\n\t"
    "s_add_u32 s57, s57, s43\n\t"
    "buffer_load_dwordx4 v[96:99], v0, s[20:23], s56 offen offset:0\n\t"
    "buffer_load_dwordx4 v[100:103], v0, s[20:23], s43 offen offset:64\n\t"
    "buffer_load_dwordx4 v[104:107], v0, s[20:23], s57 offen offset:0\n\t"
    "buffer_load_dwordx4 v[8:11], v1, s[20:23], s43 offen offset:64\n\t"
    "buffer_load_dwordx4 v[12:15], v1, s[20:23], s57 offen offset:0\n\t"
    "v_lshlrev_b32 v2, 2, v22\n\t"
    "global_load_dword v16, v2, s[28:29] offset:0\n\t"
    "global_load_dword v17, v2, s[28:29] offset:256\n\t"
    "global_load_dword v18, v2, s[28:29] offset:512\n\t"
    "v_lshrrev_b32 v0, 2, v22\n\t"
    "v_mul_u32_u24 v0, 0xc0, v0\n\t"
    "v_and_b32 v1, 3, v22\n\t"
    "v_lshl_add_u32 v0, v1, 3, v0\n\t"
    "s_lshr_b32 s78, s36, 1\n\t"
    "s_mul_i32 s78, s78, 18432\n\t"
    "s_and_b32 s59, s36, 1\n\t"
    "s_mul_i32 s59, s59, 96\n\t"
    "s_add_u32 s78, s78, s59\n\t"
    "s_and_b32 s77, s36, 1\n\t"
    "s_mul_i32 s77, s77, -1\n\t"
    "v_add_u32 v0, s78, v0\n\t"
    "v_and_b32 v1, 16, v22\n\t"
    "v_lshlrev_b32 v1, 1, v1\n\t"
    "v_xor_b32 v1, s77, v1\n\t"
    "v_subrev_u32 v1, s77, v1\n\t"
    "v_add_u32 v2, v0, v1\n\t"
    "v_sub_u32 v3, v0, v1\n\t"
    "s_mul_i32 s78, s36, 6144\n\t"
    "s_mul_i32 s59, s38, -6144\n\t"
    "s_add_u32 s78, s78, s59\n\t"
    "s_add_u32 s78, s78, 90112\n\t"
    "v_add_u32 v0, s78, v23\n\t"
    "v_cvt_pk_bf16_f32 v72, v156, v157\n\t"
    "ds_read_b64_tr_b16 v[36:37], v2\n\t"
    "v_cvt_pk_bf16_f32 v73, v158, v159\n\t"
    "ds_read_b64_tr_b16 v[38:39], v2 offset:3072\n\t"
    "v_cvt_pk_bf16_f32 v74, v160, v161\n\t"
    "ds_read_b64_tr_b16 v[40:41], v3 offset:32\n\t"
    "v_cvt_pk_bf16_f32 v75, v162, v163\n\t"
    "v_cvt_pk_bf16_f32 v76, v164, v165\n\t"
    "ds_read_b64_tr_b16 v[42:43], v3 offset:3104\n\t"
    "v_cvt_pk_bf16_f32 v77, v166, v167\n\t"
    "ds_read_b64_tr_b16 v[44:45], v2 offset:64\n\t"
    "v_cvt_pk_bf16_f32 v78, v168, v169\n\t"
    "ds_read_b64_tr_b16 v[46:47], v2 offset:3136\n\t"
    "v_cvt_pk_bf16_f32 v79, v170, v171\n\t"
    "v_cvt_pk_bf16_f32 v80, v172, v173\n\t"
    "ds_read_b64_tr_b16 v[48:49], v2 offset:6144\n\t"
    "v_cvt_pk_bf16_f32 v81, v174, v175\n\t"
    "ds_read_b64_tr_b16 v[50:51], v2 offset:9216\n\t"
    "v_cvt_pk_bf16_f32 v82, v176, v177\n\t"
    "ds_read_b64_tr_b16 v[52:53], v3 offset:6176\n\t"
    "v_cvt_pk_bf16_f32 v83, v178, v179\n\t"
    "v_cvt_pk_bf16_f32 v84, v180, v181\n\t"
    "ds_read_b64_tr_b16 v[54:55], v3 offset:9248\n\t"
    "v_cvt_pk_bf16_f32 v85, v182, v183\n\t"
    "ds_read_b64_tr_b16 v[56:57], v2 offset:6208\n\t"
    "v_cvt_pk_bf16_f32 v86, v184, v185\n\t"
    "ds_read_b64_tr_b16 v[58:59], v2 offset:9280\n\t"
    "v_cvt_pk_bf16_f32 v87, v186, v187\n\t"
    "v_cvt_pk_bf16_f32 v88, v188, v189\n\t"
    "ds_read_b64_tr_b16 v[60:61], v2 offset:12288\n\t"
    "v_cvt_pk_bf16_f32 v89, v190, v191\n\t"
    "ds_read_b64_tr_b16 v[62:63], v2 offset:15360\n\t"
    "v_cvt_pk_bf16_f32 v90, v192, v193\n\t"
    "ds_read_b64_tr_b16 v[64:65], v3 offset:12320\n\t"
    "v_cvt_pk_bf16_f32 v91, v194, v195\n\t"
    "v_cvt_pk_bf16_f32 v92, v196, v197\n\t"
    "ds_read_b64_tr_b16 v[66:67], v3 offset:15392\n\t"
    "v_cvt_pk_bf16_f32 v93, v198, v199\n\t"
    "ds_read_b64_tr_b16 v[68:69], v2 offset:12352\n\t"
    "v_cvt_pk_bf16_f32 v94, v200, v201\n\t"
    "ds_read_b64_tr_b16 v[70:71], v2 offset:15424\n\t"
    "v_cvt_pk_bf16_f32 v95, v202, v203\n\t"
    "s_waitcnt vmcnt(8)\n\t"
    "v_lshl_add_u32 v1, s66, 10, v0\n\t"
    "ds_write_b128 v1, v[72:75]\n\t"
    "v_cvt_pk_bf16_f32 v108, v108, v109\n\t"
    "v_cvt_pk_bf16_f32 v109, v110, v111\n\t"
    "v_cvt_pk_bf16_f32 v110, v112, v113\n\t"
    "v_cvt_pk_bf16_f32 v111, v114, v115\n\t"
    "v_lshl_add_u32 v1, s67, 10, v0\n\t"
    "ds_write_b128 v1, v[76:79]\n\t"
    "v_cvt_pk_bf16_f32 v112, v116, v117\n\t"
    "v_cvt_pk_bf16_f32 v113, v118, v119\n\t"
    "s_waitcnt lgkmcnt(15)\n\t"
    "v_mfma_f32_16x16x32_bf16 v[180:183], v[24:27], v[36:39], 0\n\t"
    "v_cvt_pk_bf16_f32 v114, v120, v121\n\t"
    "v_cvt_pk_bf16_f32 v115, v122, v123\n\t"
    "v_mfma_f32_16x16x32_bf16 v[184:187], v[24:27], v[40:43], 0\n\t"
    "v_lshl_add_u32 v1, s68, 10, v0\n\t"
    "ds_write_b128 v1, v[80:83]\n\t"
    "v_cvt_pk_bf16_f32 v116, v124, v125\n\t"
    "v_cvt_pk_bf16_f32 v117, v126, v127\n\t"
    "s_waitcnt lgkmcnt(15)\n\t"
    "v_mfma_f32_16x16x32_bf16 v[188:191], v[24:27], v[44:47], 0\n\t"
    "v_cvt_pk_bf16_f32 v118, v128, v129\n\t"
    "v_cvt_pk_bf16_f32 v119, v130, v131\n\t"
    "s_waitcnt lgkmcnt(13)\n\t"
    "v_mfma_f32_16x16x32_bf16 v[180:183], v[28:31], v[48:51], v[180:183]\n\t"
    "v_lshl_add_u32 v1, s69, 10, v0\n\t"
    "ds_write_b128 v1, v[84:87]\n\t"
    "v_cvt_pk_bf16_f32 v120, v132, v133\n\t"
    "v_cvt_pk_bf16_f32 v121, v134, v135\n\t"
    "s_waitcnt lgkmcnt(12)\n\t"
    "v_mfma_f32_16x16x32_bf16 v[184:187], v[28:31], v[52:55], v[184:187]\n\t"
    "v_cvt_pk_bf16_f32 v122, v136, v137\n\t"
    "v_cvt_pk_bf16_f32 v123, v138, v139\n\t"
    "s_waitcnt lgkmcnt(10)\n\t"
    "v_mfma_f32_16x16x32_bf16 v[188:191], v[28:31], v[56:59], v[188:191]\n\t"
    "v_lshl_add_u32 v1, s70, 10, v0\n\t"
    "ds_write_b128 v1, v[88:91]\n\t"
    "v_cvt_pk_bf16_f32 v124, v140, v141\n\t"
    "v_cvt_pk_bf16_f32 v125, v142, v143\n\t"
    "s_waitcnt lgkmcnt(9)\n\t"
    "v_mfma_f32_16x16x32_bf16 v[180:183], v[32:35], v[60:63], v[180:183]\n\t"
    "v_cvt_pk_bf16_f32 v126, v144, v145\n\t"
    "v_cvt_pk_bf16_f32 v127, v146, v147\n\t"
    "s_waitcnt lgkmcnt(7)\n\t"
    "v_mfma_f32_16x16x32_bf16 v[184:187], v[32:35], v[64:67], v[184:187]\n\t"
    "v_lshl_add_u32 v1, s71, 10, v0\n\t"
    "ds_write_b128 v1, v[92:95]\n\t"
    "v_cvt_pk_bf16_f32 v128, v148, v149\n\t"
    "v_cvt_pk_bf16_f32 v129, v150, v151\n\t"
    "s_waitcnt lgkmcnt(6)\n\t"
    "v_mfma_f32_16x16x32_bf16 v[188:191], v[32:35], v[68:71], v[188:191]\n\t"
    "v_cvt_pk_bf16_f32 v130, v152, v153\n\t"
    "v_cvt_pk_bf16_f32 v131, v154, v155\n\t"
    "v_cvt_pk_bf16_f32 v156, v204, v205\n\t"
    "v_cvt_pk_bf16_f32 v157, v206, v207\n\t"
    "v_cvt_pk_bf16_f32 v158, v208, v209\n\t"
    "v_cvt_pk_bf16_f32 v159, v210, v211\n\t"
    "v_cvt_pk_bf16_f32 v160, v212, v213\n\t"
    "v_cvt_pk_bf16_f32 v161, v214, v215\n\t"
    "v_cvt_pk_bf16_f32 v162, v216, v217\n\t"
    "v_cvt_pk_bf16_f32 v163, v218, v219\n\t"
    "v_cvt_pk_bf16_f32 v164, v220, v221\n\t"
    "v_cvt_pk_bf16_f32 v165, v222, v223\n\t"
    "v_cvt_pk_bf16_f32 v166, v224, v225\n\t"
    "v_cvt_pk_bf16_f32 v167, v226, v227\n\t"
    "v_cvt_pk_bf16_f32 v168, v228, v229\n\t"
    "v_cvt_pk_bf16_f32 v169, v230, v231\n\t"
    "v_cvt_pk_bf16_f32 v170, v232, v233\n\t"
    "v_cvt_pk_bf16_f32 v171, v234, v235\n\t"
    "v_cvt_pk_bf16_f32 v172, v236, v237\n\t"
    "v_cvt_pk_bf16_f32 v173, v238, v239\n\t"
    "v_cvt_pk_bf16_f32 v174, v240, v241\n\t"
    "v_cvt_pk_bf16_f32 v175, v242, v243\n\t"
    "v_cvt_pk_bf16_f32 v176, v244, v245\n\t"
    "v_cvt_pk_bf16_f32 v177, v246, v247\n\t"
    "v_cvt_pk_bf16_f32 v178, v248, v249\n\t"
    "v_cvt_pk_bf16_f32 v179, v250, v251\n\t"
    "v_cvt_pk_bf16_f32 v72, v180, v180\n\t"
    "v_and_b32 v181, 0xffff0000, v72\n\t"
    "v_sub_f32 v180, v180, v181\n\t"
    "v_cvt_pk_bf16_f32 v180, v180, v180\n\t"
    "v_bfi_b32 v72, s54, v72, v180\n\t"
    "v_mov_b32 v73, 0\n\t"
    "v_mov_b32 v74, 0\n\t"
    "v_mov_b32 v75, 0\n\t"
    "v_cvt_pk_bf16_f32 v76, v184, v184\n\t"
    "v_and_b32 v185, 0xffff0000, v76\n\t"
    "v_sub_f32 v184, v184, v185\n\t"
    "v_cvt_pk_bf16_f32 v184, v184, v184\n\t"
    "v_bfi_b32 v76, s54, v76, v184\n\t"
    "v_mov_b32 v77, 0\n\t"
    "v_mov_b32 v78, 0\n\t"
    "v_mov_b32 v79, 0\n\t"
    "v_cvt_pk_bf16_f32 v80, v188, v188\n\t"
    "v_and_b32 v189, 0xffff0000, v80\n\t"
    "v_sub_f32 v188, v188, v189\n\t"
    "v_cvt_pk_bf16_f32 v188, v188, v188\n\t"
    "v_bfi_b32 v80, s54, v80, v188\n\t"
    "v_mov_b32 v81, 0\n\t"
    "v_mov_b32 v82, 0\n\t"
    "v_mov_b32 v83, 0\n\t"
    "s_lshr_b32 s78, s36, 1\n\t"
    "s_mul_i32 s78, s78, 256\n\t"
    "s_and_b32 s59, s36, 1\n\t"
    "s_mul_i32 s59, s59, 3072\n\t"
    "s_add_u32 s78, s78, s59\n\t"
    "s_add_u32 s78, s78, 140800\n\t"
    "v_add_u32 v0, s78, v23\n\t"
    "s_mov_b32 exec_lo, 0x0000ffff\n\t"
    "s_mov_b32 exec_hi, 0x00000000\n\t"
    "ds_write_b128 v0, v[72:75]\n\t"
    "ds_write_b128 v0, v[76:79] offset:1024\n\t"
    "ds_write_b128 v0, v[80:83] offset:2048\n\t"
    "s_mov_b64 exec, -1\n\t"
    "v_cvt_pk_bf16_f32 v180, v252, v253\n\t"
    "v_cvt_pk_bf16_f32 v181, v254, v255\n\t"
    "s_cmp_lg_u32 s55, 0\n\t"
    "s_cbranch_scc1 13f\n\t"
    "s_setprio 3\n\t"
    "s_mul_i32 s77, s76, 16384\n\t"
    "s_mul_i32 s59, s36, 32\n\t"
    "s_add_u32 s77, s77, s59\n\t"
    "s_add_u32 s77, s77, 4064\n\t"
    "v_lshrrev_b32 v224, 1, v22\n\t"
    "v_lshlrev_b32 v224, 6, v224\n\t"
    "v_and_b32 v225, 1, v22\n\t"
    "v_lshl_add_u32 v224, v225, 4, v224\n\t"
    "s_mov_b32 s79, 0\n\t"
    "s_mov_b64 s[86:87], 0\n\t"
    "12:\n\t"
    "s_add_u32 s78, s77, 0\n\t"
    "buffer_load_dwordx4 v[192:195], v224, s[16:19], s78 offen sc1\n\t"
    "s_add_u32 s78, s77, 2048\n\t"
    "buffer_load_dwordx4 v[196:199], v224, s[16:19], s78 offen sc1\n\t"
    "s_add_u32 s78, s77, 4096\n\t"
    "buffer_load_dwordx4 v[200:203], v224, s[16:19], s78 offen sc1\n\t"
    "s_add_u32 s78, s77, 6144\n\t"
    "buffer_load_dwordx4 v[204:207], v224, s[16:19], s78 offen sc1\n\t"
    "s_add_u32 s78, s77, 8192\n\t"
    "buffer_load_dwordx4 v[208:211], v224, s[16:19], s78 offen sc1\n\t"
    "s_add_u32 s78, s77, 10240\n\t"
    "buffer_load_dwordx4 v[212:215], v224, s[16:19], s78 offen sc1\n\t"
    "s_add_u32 s78, s77, 12288\n\t"
    "buffer_load_dwordx4 v[216:219], v224, s[16:19], s78 offen sc1\n\t"
    "s_add_u32 s78, s77, 14336\n\t"
    "buffer_load_dwordx4 v[220:223], v224, s[16:19], s78 offen sc1\n\t"
    "s_waitcnt vmcnt(0)\n\t"
    "v_xor_b32 v192, s39, v192\n\t"
    "v_xor_b32 v194, s39, v194\n\t"
    "v_xor_b32 v227, s40, v193\n\t"
    "v_or_b32 v226, v192, v194\n\t"
    "v_bitop3_b32 v227, v227, v195, s40 bitop3:0xf6\n\t"
    "v_xor_b32 v196, s39, v196\n\t"
    "v_xor_b32 v198, s39, v198\n\t"
    "v_bitop3_b32 v227, v227, v197, s40 bitop3:0xf6\n\t"
    "v_or3_b32 v226, v226, v196, v198\n\t"
    "v_bitop3_b32 v227, v227, v199, s40 bitop3:0xf6\n\t"
    "v_xor_b32 v200, s39, v200\n\t"
    "v_xor_b32 v202, s39, v202\n\t"
    "v_bitop3_b32 v227, v227, v201, s40 bitop3:0xf6\n\t"
    "v_or3_b32 v226, v226, v200, v202\n\t"
    "v_bitop3_b32 v227, v227, v203, s40 bitop3:0xf6\n\t"
    "v_xor_b32 v204, s39, v204\n\t"
    "v_xor_b32 v206, s39, v206\n\t"
    "v_bitop3_b32 v227, v227, v205, s40 bitop3:0xf6\n\t"
    "v_or3_b32 v226, v226, v204, v206\n\t"
    "v_bitop3_b32 v227, v227, v207, s40 bitop3:0xf6\n\t"
    "v_xor_b32 v208, s39, v208\n\t"
    "v_xor_b32 v210, s39, v210\n\t"
    "v_bitop3_b32 v227, v227, v209, s40 bitop3:0xf6\n\t"
    "v_or3_b32 v226, v226, v208, v210\n\t"
    "v_bitop3_b32 v227, v227, v211, s40 bitop3:0xf6\n\t"
    "v_xor_b32 v212, s39, v212\n\t"
    "v_xor_b32 v214, s39, v214\n\t"
    "v_bitop3_b32 v227, v227, v213, s40 bitop3:0xf6\n\t"
    "v_or3_b32 v226, v226, v212, v214\n\t"
    "v_bitop3_b32 v227, v227, v215, s40 bitop3:0xf6\n\t"
    "v_xor_b32 v216, s39, v216\n\t"
    "v_xor_b32 v218, s39, v218\n\t"
    "v_bitop3_b32 v227, v227, v217, s40 bitop3:0xf6\n\t"
    "v_or3_b32 v226, v226, v216, v218\n\t"
    "v_bitop3_b32 v227, v227, v219, s40 bitop3:0xf6\n\t"
    "v_xor_b32 v220, s39, v220\n\t"
    "v_xor_b32 v222, s39, v222\n\t"
    "v_bitop3_b32 v227, v227, v221, s40 bitop3:0xf6\n\t"
    "v_or3_b32 v226, v226, v220, v222\n\t"
    "v_bitop3_b32 v227, v227, v223, s40 bitop3:0xf6\n\t"
    "v_and_b32 v226, 0x1ff, v226\n\t"
    "v_or_b32 v226, v226, v227\n\t"
    "v_cmp_ne_u32 vcc, 0, v226\n\t"
    "s_add_u32 s79, s79, 1\n\t"
    "s_cbranch_vccz 24f\n\t"
    "s_cmp_gt_u32 s79, 262144\n\t"
    "s_cbranch_scc1 24f\n\t"
    "s_mov_b64 exec, vcc\n\t"
    "s_sleep 4\n\t"
    "s_branch 12b\n\t"
    "24:\n\t"
    "s_mov_b64 exec, -1\n\t"
    "v_add_f32 v192, v192, v208\n\t"
    "v_add_f32 v194, v194, v210\n\t"
    "v_add_f32 v196, v196, v212\n\t"
    "v_add_f32 v198, v198, v214\n\t"
    "v_add_f32 v200, v200, v216\n\t"
    "v_add_f32 v202, v202, v218\n\t"
    "v_add_f32 v204, v204, v220\n\t"
    "v_add_f32 v206, v206, v222\n\t"
    "v_add_f32 v192, v192, v200\n\t"
    "v_add_f32 v194, v194, v202\n\t"
    "v_add_f32 v196, v196, v204\n\t"
    "v_add_f32 v198, v198, v206\n\t"
    "v_add_f32 v192, v192, v196\n\t"
    "v_add_f32 v194, v194, v198\n\t"
    "v_lshrrev_b32 v224, 1, v22\n\t"
    "v_add_f32_dpp v192, v192, v192 quad_perm:[2,3,0,1] row_mask:0xf bank_mask:0xf\n\t"
    "v_add_f32_dpp v194, v194, v194 quad_perm:[2,3,0,1] row_mask:0xf bank_mask:0xf\n\t"
    "v_lshlrev_b32 v224, 12, v224\n\t"
    "v_add_f32_dpp v192, v192, v192 row_ror:4 row_mask:0xf bank_mask:0xf\n\t"
    "v_add_f32_dpp v194, v194, v194 row_ror:4 row_mask:0xf bank_mask:0xf\n\t"
    "v_lshl_add_u32 v224, v225, 4, v224\n\t"
    "v_add_f32_dpp v192, v192, v192 row_ror:8 row_mask:0xf bank_mask:0xf\n\t"
    "v_add_f32_dpp v194, v194, v194 row_ror:8 row_mask:0xf bank_mask:0xf\n\t"
    "v_mov_b32 v193, v192\n\t"
    "v_mov_b32 v195, v194\n\t"
    "s_nop 0\n\t"
    "v_permlane16_swap_b32 v192, v193\n\t"
    "v_permlane16_swap_b32 v194, v195\n\t"
    "v_add_f32 v192, v192, v193\n\t"
    "v_add_f32 v194, v194, v195\n\t"
    "v_mov_b32 v193, v192\n\t"
    "v_mov_b32 v195, v194\n\t"
    "s_nop 0\n\t"
    "v_permlane32_swap_b32 v192, v193\n\t"
    "v_permlane32_swap_b32 v194, v195\n\t"
    "v_add_f32 v192, v192, v193\n\t"
    "v_add_f32 v194, v194, v195\n\t"
    "v_mov_b32 v226, 0x358637bd\n\t"
    "v_fmamk_f32 v192, v192, 0x4091afa4, v226\n\t"
    "v_fmamk_f32 v194, v194, 0x4091afa4, v226\n\t"
    "v_sqrt_f32 v192, v192\n\t"
    "v_sqrt_f32 v194, v194\n\t"
    "s_mul_i32 s78, s76, 64\n\t"
    "s_mul_i32 s59, s36, 32\n\t"
    "s_add_u32 s78, s78, s59\n\t"
    "s_add_u32 s78, s78, 790496\n\t"
    "v_add_u32 v196, 0x100, v192\n\t"
    "v_and_b32 v196, 0xfffffe00, v196\n\t"
    "v_or_b32 v196, s39, v196\n\t"
    "v_mov_b32 v197, s40\n\t"
    "v_add_u32 v198, 0x100, v194\n\t"
    "v_and_b32 v198, 0xfffffe00, v198\n\t"
    "v_or_b32 v198, s39, v198\n\t"
    "v_mov_b32 v199, s40\n\t"
    "s_mov_b32 exec_lo, 0x0000ffff\n\t"
    "s_mov_b32 exec_hi, 0x00000000\n\t"
    "buffer_store_dwordx4 v[196:199], v224, s[16:19], s78 offen sc1\n\t"
    "s_mov_b64 exec, -1\n\t"
    "s_nop 1\n\t"
    "13:\n\t"
    "s_setprio 0\n\t"
    "v_lshlrev_b32 v228, 16, v58\n\t"
    "v_and_b32 v229, 0xffff0000, v58\n\t"
    "v_lshlrev_b32 v230, 16, v59\n\t"
    "v_and_b32 v231, 0xffff0000, v59\n\t"
    "v_lshlrev_b32 v232, 16, v60\n\t"
    "v_and_b32 v233, 0xffff0000, v60\n\t"
    "v_lshlrev_b32 v234, 16, v61\n\t"
    "v_and_b32 v235, 0xffff0000, v61\n\t"
    "v_lshlrev_b32 v236, 16, v62\n\t"
    "v_and_b32 v237, 0xffff0000, v62\n\t"
    "v_lshlrev_b32 v238, 16, v63\n\t"
    "v_and_b32 v239, 0xffff0000, v63\n\t"
    "v_lshlrev_b32 v240, 16, v64\n\t"
    "v_and_b32 v241, 0xffff0000, v64\n\t"
    "v_lshlrev_b32 v242, 16, v65\n\t"
    "v_and_b32 v243, 0xffff0000, v65\n\t"
    "v_lshlrev_b32 v244, 16, v66\n\t"
    "v_and_b32 v245, 0xffff0000, v66\n\t"
    "v_lshlrev_b32 v246, 16, v67\n\t"
    "v_and_b32 v247, 0xffff0000, v67\n\t"
    "v_lshlrev_b32 v248, 16, v68\n\t"
    "v_and_b32 v249, 0xffff0000, v68\n\t"
    "v_lshlrev_b32 v250, 16, v69\n\t"
    "v_and_b32 v251, 0xffff0000, v69\n\t"
    "v_lshlrev_b32 v252, 16, v70\n\t"
    "v_and_b32 v253, 0xffff0000, v70\n\t"
    "v_lshlrev_b32 v254, 16, v71\n\t"
    "v_and_b32 v255, 0xffff0000, v71\n\t"
    "v_lshlrev_b32 v184, 16, v36\n\t"
    "v_and_b32 v185, 0xffff0000, v36\n\t"
    "v_lshlrev_b32 v186, 16, v37\n\t"
    "v_and_b32 v187, 0xffff0000, v37\n\t"
    "v_lshlrev_b32 v188, 16, v38\n\t"
    "v_and_b32 v189, 0xffff0000, v38\n\t"
    "v_lshlrev_b32 v190, 16, v39\n\t"
    "v_and_b32 v191, 0xffff0000, v39\n\t"
    "v_lshlrev_b32 v192, 16, v40\n\t"
    "v_and_b32 v193, 0xffff0000, v40\n\t"
    "v_lshlrev_b32 v194, 16, v41\n\t"
    "v_and_b32 v195, 0xffff0000, v41\n\t"
    "v_lshlrev_b32 v196, 16, v42\n\t"
    "v_and_b32 v197, 0xffff0000, v42\n\t"
    "v_lshlrev_b32 v198, 16, v43\n\t"
    "v_and_b32 v199, 0xffff0000, v43\n\t"
    "v_lshlrev_b32 v200, 16, v44\n\t"
    "v_and_b32 v201, 0xffff0000, v44\n\t"
    "v_lshlrev_b32 v202, 16, v45\n\t"
    "v_and_b32 v203, 0xffff0000, v45\n\t"
    "v_lshlrev_b32 v204, 16, v46\n\t"
    "v_and_b32 v205, 0xffff0000, v46\n\t"
    "v_lshlrev_b32 v206, 16, v47\n\t"
    "v_and_b32 v207, 0xffff0000, v47\n\t"
    "v_lshlrev_b32 v208, 16, v48\n\t"
    "v_and_b32 v209, 0xffff0000, v48\n\t"
    "v_lshlrev_b32 v210, 16, v49\n\t"
    "v_and_b32 v211, 0xffff0000, v49\n\t"
    "v_lshlrev_b32 v212, 16, v50\n\t"
    "v_and_b32 v213, 0xffff0000, v50\n\t"
    "v_lshlrev_b32 v214, 16, v51\n\t"
    "v_and_b32 v215, 0xffff0000, v51\n\t"
    "v_lshlrev_b32 v216, 16, v52\n\t"
    "v_and_b32 v217, 0xffff0000, v52\n\t"
    "v_lshlrev_b32 v218, 16, v53\n\t"
    "v_and_b32 v219, 0xffff0000, v53\n\t"
    "v_lshlrev_b32 v220, 16, v54\n\t"
    "v_and_b32 v221, 0xffff0000, v54\n\t"
    "v_lshlrev_b32 v222, 16, v55\n\t"
    "v_and_b32 v223, 0xffff0000, v55\n\t"
    "v_lshlrev_b32 v224, 16, v56\n\t"
    "v_and_b32 v225, 0xffff0000, v56\n\t"
    "v_lshlrev_b32 v226, 16, v57\n\t"
    "v_and_b32 v227, 0xffff0000, v57\n\t"
    "s_mul_i32 s78, s38, -4096\n\t"
    "s_add_u32 s78, s78, 4096\n\t"
    "v_add_u32 v2, s78, v23\n\t"
    "s_mul_i32 s78, s66, 6144\n\t"
    "v_add_u32 v48, s78, v2\n\t"
    "s_mul_i32 s78, s67, 6144\n\t"
    "v_add_u32 v49, s78, v2\n\t"
    "s_mul_i32 s78, s68, 6144\n\t"
    "v_add_u32 v50, s78, v2\n\t"
    "s_mul_i32 s78, s69, 6144\n\t"
    "v_add_u32 v51, s78, v2\n\t"
    "s_mul_i32 s78, s70, 6144\n\t"
    "v_add_u32 v52, s78, v2\n\t"
    "s_mul_i32 s78, s71, 6144\n\t"
    "v_add_u32 v53, s78, v2\n\t"
    "s_mov_b32 s77, s36\n\t"
    "s_cmp_gt_u32 s77, 3\n\t"
    "s_cbranch_scc1 18f\n\t"
    "v_and_b32 v0, 15, v22\n\t"
    "v_min_u32 v0, 11, v0\n\t"
    "v_lshrrev_b32 v1, 2, v19\n\t"
    "v_lshl_add_u32 v54, v0, 4, v1\n\t"
    "s_lshr_b32 s78, s77, 1\n\t"
    "s_mul_i32 s78, s78, 43008\n\t"
    "s_and_b32 s79, s77, 1\n\t"
    "s_mul_i32 s79, s79, 192\n\t"
    "s_add_u32 s78, s78, s79\n\t"
    "s_add_u32 s78, s78, 518400\n\t"
    "v_add_u32 v54, s78, v54\n\t"
    "buffer_load_dword v57, v54, s[20:23], s43 offen\n\t"
    "v_mul_u32_u24 v55, 96, v19\n\t"
    "v_lshl_add_u32 v55, v0, 7, v55\n\t"
    "s_lshl_b32 s78, s77, 4\n\t"
    "s_add_u32 s78, s78, 114688\n\t"
    "v_add_u32 v55, s78, v55\n\t"
    "v_lshlrev_b32 v56, 1, v1\n\t"
    "v_lshl_add_u32 v56, v0, 5, v56\n\t"
    "v_add_u32 v56, 147008, v56\n\t"
    "18:\n\t"
    "v_and_b32 v0, 15, v22\n\t"
    "v_lshrrev_b32 v1, 1, v19\n\t"
    "v_sub_u32 v0, v0, v1\n\t"
    "v_and_b32 v1, 1, v22\n\t"
    "v_lshlrev_b32 v1, 4, v1\n\t"
    "v_mov_b32 v2, 0x3f80\n\t"
    "v_lshlrev_b32 v1, v1, v2\n\t"
    "v_ashrrev_i32 v0, 1, v0\n\t"
    "v_cmp_eq_u32 s[44:45], 0, v0\n\t"
    "v_cmp_eq_u32 s[46:47], 1, v0\n\t"
    "v_cmp_eq_u32 s[48:49], 2, v0\n\t"
    "v_cmp_eq_u32 s[80:81], 3, v0\n\t"
    "v_cmp_eq_u32 s[82:83], -8, v0\n\t"
    "v_cmp_eq_u32 s[84:85], -7, v0\n\t"
    "v_cmp_eq_u32 s[60:61], -6, v0\n\t"
    "v_cmp_eq_u32 s[62:63], -5, v0\n\t"
    "v_mov_b32 v24, 0x3f803f80\n\t"
    "v_mov_b32 v25, 0x3f803f80\n\t"
    "v_mov_b32 v26, 0x3f803f80\n\t"
    "v_mov_b32 v27, 0x3f803f80\n\t"
    "v_cndmask_b32 v28, 0, v1, s[44:45]\n\t"
    "v_cndmask_b32 v29, 0, v1, s[46:47]\n\t"
    "v_cndmask_b32 v30, 0, v1, s[48:49]\n\t"
    "v_cndmask_b32 v31, 0, v1, s[80:81]\n\t"
    "v_cndmask_b32 v32, 0, v1, s[82:83]\n\t"
    "v_cndmask_b32 v33, 0, v1, s[84:85]\n\t"
    "v_cndmask_b32 v34, 0, v1, s[60:61]\n\t"
    "v_cndmask_b32 v35, 0, v1, s[62:63]\n\t"
    "v_mov_b32 v2, 0\n\t"
    "s_mul_i32 s78, s36, 4\n\t"
    "s_add_u32 s78, s78, 146944\n\t"
    "v_mov_b32 v3, s78\n\t"
    "ds_write_b32 v3, v2\n\t"
    "ds_write_b32 v3, v2 offset:32\n\t"
    "s_waitcnt lgkmcnt(0)\n\t"
    "s_barrier\n\t"
    "s_xor_b32 s78, s36, 4\n\t"
    "s_mul_i32 s78, s78, 6144\n\t"
    "s_add_u32 s78, s78, 90112\n\t"
    "v_add_u32 v0, s78, v23\n\t"
    "v_add_u32 v1, 140800, v23\n\t"
    "s_mul_i32 s78, s38, -4096\n\t"
    "s_add_u32 s78, s78, 4096\n\t"
    "v_add_u32 v2, s78, v1\n\t"
    "s_mul_i32 s78, s38, 4096\n\t"
    "v_add_u32 v3, s78, v1\n\t"
    "ds_read_b128 v[36:39], v2\n\t"
    "ds_read_b128 v[40:43], v2 offset:1024\n\t"
    "ds_read_b128 v[44:47], v1 offset:2048\n\t"
    "ds_read_b128 v[4:7], v1 offset:3072\n\t"
    "ds_read_b128 v[84:87], v3\n\t"
    "ds_read_b128 v[88:91], v3 offset:1024\n\t"
    "ds_read_b128 v[132:135], v0\n\t"
    "ds_read_b128 v[136:139], v0 offset:1024\n\t"
    "ds_read_b128 v[140:143], v0 offset:2048\n\t"
    "ds_read_b128 v[144:147], v0 offset:3072\n\t"
    "ds_read_b128 v[148:151], v0 offset:4096\n\t"
    "ds_read_b128 v[152:155], v0 offset:5120\n\t"
    "s_waitcnt vmcnt(0)\n\t"
    "v_mfma_f32_16x16x32_bf16 v[60:63], v[28:31], v[96:99], 0\n\t"
    "v_mfma_f32_16x16x32_bf16 v[64:67], v[32:35], v[96:99], 0\n\t"
    "v_mfma_f32_16x16x32_bf16 v[68:71], v[28:31], v[100:103], 0\n\t"
    "v_mfma_f32_16x16x32_bf16 v[72:75], v[32:35], v[100:103], 0\n\t"
    "v_mfma_f32_16x16x32_bf16 v[76:79], v[28:31], v[104:107], 0\n\t"
    "v_mfma_f32_16x16x32_bf16 v[80:83], v[32:35], v[104:107], 0\n\t"
    "s_waitcnt lgkmcnt(11)\n\t"
    "v_mfma_f32_16x16x32_bf16 v[60:63], v[36:39], v[24:27], v[60:63]\n\t"
    "s_waitcnt lgkmcnt(10)\n\t"
    "v_mfma_f32_16x16x32_bf16 v[64:67], v[40:43], v[24:27], v[64:67]\n\t"
    "s_waitcnt lgkmcnt(9)\n\t"
    "v_mfma_f32_16x16x32_bf16 v[68:71], v[44:47], v[24:27], v[68:71]\n\t"
    "s_waitcnt lgkmcnt(8)\n\t"
    "v_mfma_f32_16x16x32_bf16 v[72:75], v[4:7], v[24:27], v[72:75]\n\t"
    "s_waitcnt lgkmcnt(7)\n\t"
    "v_mfma_f32_16x16x32_bf16 v[76:79], v[84:87], v[24:27], v[76:79]\n\t"
    "s_waitcnt lgkmcnt(6)\n\t"
    "v_mfma_f32_16x16x32_bf16 v[80:83], v[88:91], v[24:27], v[80:83]\n\t"
    "s_cmp_lg_u32 s36, 7\n\t"
    "s_cbranch_scc1 39f\n\t"
    "v_add_u32 v54, 147008, v19\n\t"
    "v_mfma_f32_16x16x32_bf16 v[56:59], v[36:39], v[24:27], 0\n\t"
    "s_mov_b32 exec_lo, 0x00010001\n\t"
    "s_mov_b32 exec_hi, 0x00010001\n\t"
    "s_nop 5\n\t"
    "ds_write_b128 v54, v[56:59]\n\t"
    "s_mov_b64 exec, -1\n\t"
    "v_mfma_f32_16x16x32_bf16 v[56:59], v[40:43], v[24:27], 0\n\t"
    "s_mov_b32 exec_lo, 0x00010001\n\t"
    "s_mov_b32 exec_hi, 0x00010001\n\t"
    "s_nop 5\n\t"
    "ds_write_b128 v54, v[56:59] offset:64\n\t"
    "s_mov_b64 exec, -1\n\t"
    "v_mfma_f32_16x16x32_bf16 v[56:59], v[44:47], v[24:27], 0\n\t"
    "s_mov_b32 exec_lo, 0x00010001\n\t"
    "s_mov_b32 exec_hi, 0x00010001\n\t"
    "s_nop 5\n\t"
    "ds_write_b128 v54, v[56:59] offset:128\n\t"
    "s_mov_b64 exec, -1\n\t"
    "v_mfma_f32_16x16x32_bf16 v[56:59], v[4:7], v[24:27], 0\n\t"
    "s_mov_b32 exec_lo, 0x00010001\n\t"
    "s_mov_b32 exec_hi, 0x00010001\n\t"
    "s_nop 5\n\t"
    "ds_write_b128 v54, v[56:59] offset:192\n\t"
    "s_mov_b64 exec, -1\n\t"
    "v_mfma_f32_16x16x32_bf16 v[56:59], v[84:87], v[24:27], 0\n\t"
    "s_mov_b32 exec_lo, 0x00010001\n\t"
    "s_mov_b32 exec_hi, 0x00010001\n\t"
    "s_nop 5\n\t"
    "ds_write_b128 v54, v[56:59] offset:256\n\t"
    "s_mov_b64 exec, -1\n\t"
    "v_mfma_f32_16x16x32_bf16 v[56:59], v[88:91], v[24:27], 0\n\t"
    "s_mov_b32 exec_lo, 0x00010001\n\t"
    "s_mov_b32 exec_hi, 0x00010001\n\t"
    "s_nop 5\n\t"
    "ds_write_b128 v54, v[56:59] offset:320\n\t"
    "s_mov_b64 exec, -1\n\t"
    "39:\n\t"
    "v_mfma_f32_16x16x32_bf16 v[100:103], v[28:31], v[12:15], 0\n\t"
    "v_mfma_f32_16x16x32_bf16 v[104:107], v[32:35], v[12:15], 0\n\t"
    "v_mfma_f32_16x16x32_bf16 v[100:103], v[84:87], v[24:27], v[100:103]\n\t"
    "v_mfma_f32_16x16x32_bf16 v[104:107], v[88:91], v[24:27], v[104:107]\n\t"
    "s_cmp_eq_u32 s38, 0\n\t"
    "s_cbranch_scc0 20f\n\t"
    "v_mfma_f32_16x16x32_bf16 v[92:95], v[28:31], v[8:11], 0\n\t"
    "v_mov_b32 v96, 0\n\t"
    "v_mov_b32 v97, 0\n\t"
    "v_mov_b32 v98, 0\n\t"
    "v_mov_b32 v99, 0\n\t"
    "v_mfma_f32_16x16x32_bf16 v[92:95], v[44:47], v[24:27], v[92:95]\n\t"
    "s_branch 21f\n\t"
    "20:\n\t"
    "v_mfma_f32_16x16x32_bf16 v[96:99], v[32:35], v[8:11], 0\n\t"
    "v_mov_b32 v92, 0\n\t"
    "v_mov_b32 v93, 0\n\t"
    "v_mov_b32 v94, 0\n\t"
    "v_mov_b32 v95, 0\n\t"
    "v_mfma_f32_16x16x32_bf16 v[96:99], v[4:7], v[24:27], v[96:99]\n\t"
    "21:\n\t"
    "s_cmp_lg_u32 s36, 0\n\t"
    "s_cbranch_scc1 36f\n\t"
    "s_and_b32 s77, s42, 7\n\t"
    "s_mul_i32 s77, s77, 4096\n\t"
    "s_add_u32 s77, s77, 790528\n\t"
    "buffer_load_dwordx4 v[36:39], v23, s[16:19], s77 offen offset:0 sc1\n\t"
    "buffer_load_dwordx4 v[40:43], v23, s[16:19], s77 offen offset:1024 sc1\n\t"
    "buffer_load_dwordx4 v[44:47], v23, s[16:19], s77 offen offset:2048 sc1\n\t"
    "36:\n\t"
    "v_mov_b32_dpp v182, v180 row_shl:4 row_mask:0xf bank_mask:0xf\n\t"
    "v_mov_b32_dpp v183, v181 row_shl:4 row_mask:0xf bank_mask:0xf\n\t"
    "s_lshr_b32 s78, s36, 1\n\t"
    "s_mul_i32 s78, s78, 384\n\t"
    "s_add_u32 s78, s78, 139264\n\t"
    "v_add_u32 v0, s78, v19\n\t"
    "s_lshr_b32 s78, s36, 1\n\t"
    "s_mul_i32 s78, s78, 18432\n\t"
    "s_and_b32 s59, s36, 1\n\t"
    "s_mul_i32 s59, s59, 3072\n\t"
    "s_add_u32 s78, s78, s59\n\t"
    "v_add_u32 v1, s78, v23\n\t"
    "s_mul_i32 s78, s38, -40960\n\t"
    "s_add_u32 s78, s78, 40960\n\t"
    "v_add_u32 v3, s78, v23\n\t"
    "v_and_b32 v6, 3, v22\n\t"
    "v_lshl_add_u32 v6, v6, 2, v19\n\t"
    "v_and_b32 v7, 4, v22\n\t"
    "v_lshl_add_u32 v6, v7, 4, v6\n\t"
    "v_lshlrev_b32 v6, 2, v6\n\t"
    "s_mul_i32 s78, s36, 1536\n\t"
    "s_mul_i32 s59, s38, -1024\n\t"
    "s_add_u32 s78, s78, s59\n\t"
    "s_add_u32 s78, s78, 115712\n\t"
    "v_add_u32 v6, s78, v6\n\t"
    "s_mul_i32 s78, s36, 3072\n\t"
    "s_add_u32 s78, s78, 90112\n\t"
    "v_add_u32 v4, s78, v23\n\t"
    "s_xor_b32 s78, s36, 4\n\t"
    "s_mul_i32 s78, s78, 3072\n\t"
    "s_add_u32 s78, s78, 90112\n\t"
    "v_add_u32 v5, s78, v23\n\t"
    "v_and_b32 v7, 16, v22\n\t"
    "v_lshlrev_b32 v7, 1, v7\n\t"
    "v_lshrrev_b32 v9, 1, v22\n\t"
    "v_and_b32 v8, 16, v9\n\t"
    "v_add_u32 v7, v7, v8\n\t"
    "v_add_u32 v8, v21, v7\n\t"
    "v_add_u32 v7, v20, v7\n\t"
    "v_and_b32 v9, 24, v9\n\t"
    "s_mul_i32 s78, s38, 32\n\t"
    "v_add3_u32 v9, v21, v9, s78\n\t"
    "s_mul_i32 s78, s36, 4\n\t"
    "s_add_u32 s78, s78, 146944\n\t"
    "v_mov_b32 v15, s78\n\t"
    "v_xor_b32 v2, 16, v15\n\t"
    "s_mul_i32 s55, s38, 4096\n\t"
    "s_add_u32 s55, s55, -2048\n\t"
    "s_ashr_i32 s76, s55, 2\n\t"
    "s_cmp_lg_u32 s32, 0\n\t"
    "s_cbranch_scc1 14f\n\t"
    "s_mov_b32 s79, 0\n\t"
    "s_mov_b64 s[86:87], 0\n\t"
    "s_setprio 3\n\t"
    "v_lshlrev_b32 v84, 16, v16\n\t"
    "v_and_b32 v85, 0xffff0000, v16\n\t"
    "v_lshlrev_b32 v86, 16, v17\n\t"
    "v_and_b32 v87, 0xffff0000, v17\n\t"
    "v_lshlrev_b32 v88, 16, v18\n\t"
    "v_and_b32 v89, 0xffff0000, v18\n\t"
    "v_mov_b32 v12, 0x34fb9d56\n\t"
    "v_mov_b32 v90, 0x40088e99\n\t"
    "v_lshrrev_b32 v14, 1, v23\n\t"
    "v_add_u32 v14, 139264, v14\n\t"
    "s_branch 35f\n\t"
    "15:\n\t"
    "buffer_load_dwordx4 v[36:39], v23, s[16:19], s77 offen offset:0 sc1\n\t"
    "buffer_load_dwordx4 v[40:43], v23, s[16:19], s77 offen offset:1024 sc1\n\t"
    "buffer_load_dwordx4 v[44:47], v23, s[16:19], s77 offen offset:2048 sc1\n\t"
    "35:\n\t"
    "s_waitcnt vmcnt(0)\n\t"
    "v_xor_b32 v36, s39, v36\n\t"
    "v_xor_b32 v38, s39, v38\n\t"
    "v_xor_b32 v11, s40, v37\n\t"
    "v_or_b32 v10, v36, v38\n\t"
    "v_bitop3_b32 v11, v11, v39, s40 bitop3:0xf6\n\t"
    "v_xor_b32 v40, s39, v40\n\t"
    "v_xor_b32 v42, s39, v42\n\t"
    "v_bitop3_b32 v11, v11, v41, s40 bitop3:0xf6\n\t"
    "v_or3_b32 v10, v10, v40, v42\n\t"
    "v_bitop3_b32 v11, v11, v43, s40 bitop3:0xf6\n\t"
    "v_xor_b32 v44, s39, v44\n\t"
    "v_xor_b32 v46, s39, v46\n\t"
    "v_bitop3_b32 v11, v11, v45, s40 bitop3:0xf6\n\t"
    "v_or3_b32 v10, v10, v44, v46\n\t"
    "v_bitop3_b32 v11, v11, v47, s40 bitop3:0xf6\n\t"
    "v_and_b32 v10, 0x1ff, v10\n\t"
    "v_or_b32 v10, v10, v11\n\t"
    "v_cmp_ne_u32 vcc, 0, v10\n\t"
    "s_add_u32 s79, s79, 1\n\t"
    "s_cbranch_vccz 16f\n\t"
    "s_cmp_gt_u32 s79, 262144\n\t"
    "s_cbranch_scc1 16f\n\t"
    "s_mov_b64 exec, vcc\n\t"
    "s_sleep 4\n\t"
    "s_branch 15b\n\t"
    "16:\n\t"
    "s_mov_b64 exec, -1\n\t"
    "v_add_f32 v10, v36, v38\n\t"
    "v_add_f32 v11, v40, v42\n\t"
    "v_add_f32 v13, v44, v46\n\t"
    "v_add_f32 v10, v10, v11\n\t"
    "v_add_f32 v10, v10, v13\n\t"
    "v_mul_f32 v84, v84, v36\n\t"
    "v_mul_f32 v85, v85, v38\n\t"
    "v_add_f32_dpp v10, v10, v10 quad_perm:[1,0,3,2] row_mask:0xf bank_mask:0xf\n\t"
    "v_mul_f32 v86, v86, v40\n\t"
    "v_mul_f32 v87, v87, v42\n\t"
    "v_add_f32_dpp v10, v10, v10 quad_perm:[2,3,0,1] row_mask:0xf bank_mask:0xf\n\t"
    "v_mul_f32 v88, v88, v44\n\t"
    "s_nop 0\n\t"
    "v_add_f32_dpp v10, v10, v10 row_ror:4 row_mask:0xf bank_mask:0xf\n\t"
    "v_mul_f32 v89, v89, v46\n\t"
    "s_nop 0\n\t"
    "v_add_f32_dpp v10, v10, v10 row_ror:8 row_mask:0xf bank_mask:0xf\n\t"
    "v_mov_b32 v11, v10\n\t"
    "s_nop 0\n\t"
    "s_nop 0\n\t"
    "v_permlane16_swap_b32 v10, v11\n\t"
    "v_add_f32 v10, v10, v11\n\t"
    "v_mov_b32 v11, v10\n\t"
    "s_nop 0\n\t"
    "s_nop 0\n\t"
    "v_permlane32_swap_b32 v10, v11\n\t"
    "v_add_f32 v10, v10, v11\n\t"
    "v_fmamk_f32 v10, v10, 0x3a9ff8e5, v12\n\t"
    "v_rcp_f32 v10, v10\n\t"
    "s_nop 0\n\t"
    "v_pk_fma_f32 v[84:85], v[84:85], v[10:11], v[90:91] op_sel_hi:[1,0,0]\n\t"
    "ds_write_b64 v14, v[84:85]\n\t"
    "v_pk_fma_f32 v[86:87], v[86:87], v[10:11], v[90:91] op_sel_hi:[1,0,0]\n\t"
    "ds_write_b64 v14, v[86:87] offset:512\n\t"
    "v_pk_fma_f32 v[88:89], v[88:89], v[10:11], v[90:91] op_sel_hi:[1,0,0]\n\t"
    "ds_write_b64 v14, v[88:89] offset:1024\n\t"
    "s_setprio 0\n\t"
    "s_cmp_lg_u32 s42, 0\n\t"
    "s_cbranch_scc1 14f\n\t"
    "v_mov_b32 v10, s41\n\t"
    "v_mov_b32 v11, s40\n\t"
    "s_mov_b32 exec_lo, 0x00000001\n\t"
    "s_mov_b32 exec_hi, 0x00000000\n\t"
    "buffer_store_dwordx2 v[10:11], off, s[16:19], 0 sc1\n\t"
    "s_mov_b64 exec, -1\n\t"
    "14:\n\t"
    "s_waitcnt lgkmcnt(0)\n\t"
    "s_barrier\n\t"
    "ds_read_b128 v[24:27], v0\n\t"
    "ds_read_b128 v[28:31], v0 offset:64\n\t"
    "ds_read_b128 v[32:35], v0 offset:128\n\t"
    "ds_read_b128 v[36:39], v0 offset:192\n\t"
    "ds_read_b128 v[40:43], v0 offset:256\n\t"
    "ds_read_b128 v[44:47], v0 offset:320\n\t"
    "s_waitcnt lgkmcnt(5)\n\t"
    "v_pk_mul_f32 v[184:185], v[184:185], v[24:25]\n\t"
    "v_pk_mul_f32 v[186:187], v[186:187], v[26:27]\n\t"
    "s_waitcnt lgkmcnt(4)\n\t"
    "v_pk_mul_f32 v[188:189], v[188:189], v[28:29]\n\t"
    "v_pk_mul_f32 v[190:191], v[190:191], v[30:31]\n\t"
    "v_cvt_pk_bf16_f32 v184, v184, v185\n\t"
    "v_cvt_pk_bf16_f32 v185, v186, v187\n\t"
    "v_cvt_pk_bf16_f32 v186, v188, v189\n\t"
    "v_cvt_pk_bf16_f32 v187, v190, v191\n\t"
    "ds_write_b128 v1, v[184:187]\n\t"
    "v_pk_mul_f32 v[192:193], v[192:193], v[24:25]\n\t"
    "v_pk_mul_f32 v[194:195], v[194:195], v[26:27]\n\t"
    "v_pk_mul_f32 v[196:197], v[196:197], v[28:29]\n\t"
    "v_pk_mul_f32 v[198:199], v[198:199], v[30:31]\n\t"
    "v_cvt_pk_bf16_f32 v192, v192, v193\n\t"
    "v_cvt_pk_bf16_f32 v193, v194, v195\n\t"
    "v_cvt_pk_bf16_f32 v194, v196, v197\n\t"
    "v_cvt_pk_bf16_f32 v195, v198, v199\n\t"
    "ds_write_b128 v1, v[192:195] offset:1024\n\t"
    "v_pk_mul_f32 v[200:201], v[200:201], v[24:25]\n\t"
    "v_pk_mul_f32 v[202:203], v[202:203], v[26:27]\n\t"
    "v_pk_mul_f32 v[204:205], v[204:205], v[28:29]\n\t"
    "v_pk_mul_f32 v[206:207], v[206:207], v[30:31]\n\t"
    "v_cvt_pk_bf16_f32 v200, v200, v201\n\t"
    "v_cvt_pk_bf16_f32 v201, v202, v203\n\t"
    "v_cvt_pk_bf16_f32 v202, v204, v205\n\t"
    "v_cvt_pk_bf16_f32 v203, v206, v207\n\t"
    "ds_write_b128 v1, v[200:203] offset:2048\n\t"
    "s_waitcnt lgkmcnt(6)\n\t"
    "v_pk_mul_f32 v[208:209], v[208:209], v[32:33]\n\t"
    "v_pk_mul_f32 v[210:211], v[210:211], v[34:35]\n\t"
    "s_waitcnt lgkmcnt(5)\n\t"
    "v_pk_mul_f32 v[212:213], v[212:213], v[36:37]\n\t"
    "v_pk_mul_f32 v[214:215], v[214:215], v[38:39]\n\t"
    "v_cvt_pk_bf16_f32 v208, v208, v209\n\t"
    "v_cvt_pk_bf16_f32 v209, v210, v211\n\t"
    "v_cvt_pk_bf16_f32 v210, v212, v213\n\t"
    "v_cvt_pk_bf16_f32 v211, v214, v215\n\t"
    "ds_write_b128 v1, v[208:211] offset:6144\n\t"
    "v_pk_mul_f32 v[216:217], v[216:217], v[32:33]\n\t"
    "v_pk_mul_f32 v[218:219], v[218:219], v[34:35]\n\t"
    "v_pk_mul_f32 v[220:221], v[220:221], v[36:37]\n\t"
    "v_pk_mul_f32 v[222:223], v[222:223], v[38:39]\n\t"
    "v_cvt_pk_bf16_f32 v216, v216, v217\n\t"
    "v_cvt_pk_bf16_f32 v217, v218, v219\n\t"
    "v_cvt_pk_bf16_f32 v218, v220, v221\n\t"
    "v_cvt_pk_bf16_f32 v219, v222, v223\n\t"
    "ds_write_b128 v1, v[216:219] offset:7168\n\t"
    "v_pk_mul_f32 v[224:225], v[224:225], v[32:33]\n\t"
    "v_pk_mul_f32 v[226:227], v[226:227], v[34:35]\n\t"
    "v_pk_mul_f32 v[228:229], v[228:229], v[36:37]\n\t"
    "v_pk_mul_f32 v[230:231], v[230:231], v[38:39]\n\t"
    "v_cvt_pk_bf16_f32 v224, v224, v225\n\t"
    "v_cvt_pk_bf16_f32 v225, v226, v227\n\t"
    "v_cvt_pk_bf16_f32 v226, v228, v229\n\t"
    "v_cvt_pk_bf16_f32 v227, v230, v231\n\t"
    "ds_write_b128 v1, v[224:227] offset:8192\n\t"
    "s_waitcnt lgkmcnt(7)\n\t"
    "v_pk_mul_f32 v[232:233], v[232:233], v[40:41]\n\t"
    "v_pk_mul_f32 v[234:235], v[234:235], v[42:43]\n\t"
    "s_waitcnt lgkmcnt(6)\n\t"
    "v_pk_mul_f32 v[236:237], v[236:237], v[44:45]\n\t"
    "v_pk_mul_f32 v[238:239], v[238:239], v[46:47]\n\t"
    "v_cvt_pk_bf16_f32 v232, v232, v233\n\t"
    "v_cvt_pk_bf16_f32 v233, v234, v235\n\t"
    "v_cvt_pk_bf16_f32 v234, v236, v237\n\t"
    "v_cvt_pk_bf16_f32 v235, v238, v239\n\t"
    "ds_write_b128 v1, v[232:235] offset:12288\n\t"
    "v_pk_mul_f32 v[240:241], v[240:241], v[40:41]\n\t"
    "v_pk_mul_f32 v[242:243], v[242:243], v[42:43]\n\t"
    "v_pk_mul_f32 v[244:245], v[244:245], v[44:45]\n\t"
    "v_pk_mul_f32 v[246:247], v[246:247], v[46:47]\n\t"
    "v_cvt_pk_bf16_f32 v240, v240, v241\n\t"
    "v_cvt_pk_bf16_f32 v241, v242, v243\n\t"
    "v_cvt_pk_bf16_f32 v242, v244, v245\n\t"
    "v_cvt_pk_bf16_f32 v243, v246, v247\n\t"
    "ds_write_b128 v1, v[240:243] offset:13312\n\t"
    "v_pk_mul_f32 v[248:249], v[248:249], v[40:41]\n\t"
    "v_pk_mul_f32 v[250:251], v[250:251], v[42:43]\n\t"
    "v_pk_mul_f32 v[252:253], v[252:253], v[44:45]\n\t"
    "v_pk_mul_f32 v[254:255], v[254:255], v[46:47]\n\t"
    "v_cvt_pk_bf16_f32 v248, v248, v249\n\t"
    "v_cvt_pk_bf16_f32 v249, v250, v251\n\t"
    "v_cvt_pk_bf16_f32 v250, v252, v253\n\t"
    "v_cvt_pk_bf16_f32 v251, v254, v255\n\t"
    "ds_write_b128 v1, v[248:251] offset:14336\n\t"
    "s_waitcnt lgkmcnt(0)\n\t"
    "s_barrier\n\t"
    "ds_read_b128 v[184:187], v48\n\t"
    "ds_read_b128 v[188:191], v48 offset:1024\n\t"
    "ds_read_b128 v[192:195], v3\n\t"
    "ds_read_b128 v[196:199], v3 offset:1024\n\t"
    "ds_read_b128 v[200:203], v49\n\t"
    "ds_read_b128 v[204:207], v49 offset:1024\n\t"
    "ds_read_b128 v[208:211], v3 offset:6144\n\t"
    "ds_read_b128 v[212:215], v3 offset:7168\n\t"
    "ds_read_b128 v[216:219], v50\n\t"
    "ds_read_b128 v[220:223], v50 offset:1024\n\t"
    "v_mov_b32_dpp v16, v180 row_shl:8 row_mask:0xf bank_mask:0xf\n\t"
    "v_mov_b32_dpp v18, v180 row_shl:12 row_mask:0xf bank_mask:0xf\n\t"
    "v_mov_b32_dpp v17, v181 row_shl:8 row_mask:0xf bank_mask:0xf\n\t"
    "v_mov_b32_dpp v19, v181 row_shl:12 row_mask:0xf bank_mask:0xf\n\t"
    "s_setprio 3\n\t"
    "s_waitcnt lgkmcnt(6)\n\t"
    "v_mfma_f32_16x16x32_bf16 v[60:63], v[184:187], v[108:111], v[60:63]\n\t"
    "v_mfma_f32_16x16x32_bf16 v[84:87], v[184:187], v[156:159], 0\n\t"
    "v_mfma_f32_16x16x32_bf16 v[232:235], v[184:187], v[180:183], 0\n\t"
    "ds_read_b128 v[224:227], v3 offset:12288\n\t"
    "v_mfma_f32_16x16x32_bf16 v[64:67], v[188:191], v[108:111], v[64:67]\n\t"
    "v_mfma_f32_16x16x32_bf16 v[88:91], v[188:191], v[156:159], 0\n\t"
    "v_mfma_f32_16x16x32_bf16 v[236:239], v[188:191], v[180:183], 0\n\t"
    "ds_read_b128 v[228:231], v3 offset:13312\n\t"
    "v_mfma_f32_16x16x32_bf16 v[60:63], v[192:195], v[132:135], v[60:63]\n\t"
    "ds_read_b128 v[184:187], v51\n\t"
    "v_mfma_f32_16x16x32_bf16 v[64:67], v[196:199], v[132:135], v[64:67]\n\t"
    "ds_read_b128 v[188:191], v51 offset:1024\n\t"
    "s_waitcnt lgkmcnt(6)\n\t"
    "v_mfma_f32_16x16x32_bf16 v[60:63], v[200:203], v[112:115], v[60:63]\n\t"
    "v_mfma_f32_16x16x32_bf16 v[84:87], v[200:203], v[160:163], v[84:87]\n\t"
    "v_mfma_f32_16x16x32_bf16 v[64:67], v[204:207], v[112:115], v[64:67]\n\t"
    "v_mfma_f32_16x16x32_bf16 v[88:91], v[204:207], v[160:163], v[88:91]\n\t"
    "s_cmp_eq_u32 s37, 0\n\t"
    "s_cbranch_scc1 4f\n\t"
    "v_mfma_f32_16x16x32_bf16 v[232:235], v[200:203], v[16:19], v[232:235]\n\t"
    "v_mfma_f32_16x16x32_bf16 v[236:239], v[204:207], v[16:19], v[236:239]\n\t"
    "4:\n\t"
    "ds_read_b128 v[192:195], v3 offset:18432\n\t"
    "ds_read_b128 v[196:199], v3 offset:19456\n\t"
    "v_mfma_f32_16x16x32_bf16 v[60:63], v[208:211], v[136:139], v[60:63]\n\t"
    "ds_read_b128 v[200:203], v52\n\t"
    "v_mfma_f32_16x16x32_bf16 v[64:67], v[212:215], v[136:139], v[64:67]\n\t"
    "ds_read_b128 v[204:207], v52 offset:1024\n\t"
    "s_waitcnt lgkmcnt(6)\n\t"
    "v_mfma_f32_16x16x32_bf16 v[60:63], v[216:219], v[116:119], v[60:63]\n\t"
    "v_mfma_f32_16x16x32_bf16 v[84:87], v[216:219], v[164:167], v[84:87]\n\t"
    "ds_read_b128 v[208:211], v3 offset:24576\n\t"
    "v_mfma_f32_16x16x32_bf16 v[64:67], v[220:223], v[116:119], v[64:67]\n\t"
    "v_mov_b32_dpp v232, v236 row_shr:4 row_mask:0xf bank_mask:0x2\n\t"
    "v_mov_b32_dpp v233, v237 row_shr:4 row_mask:0xf bank_mask:0x2\n\t"
    "v_mov_b32_dpp v234, v238 row_shr:4 row_mask:0xf bank_mask:0x2\n\t"
    "v_mov_b32_dpp v235, v239 row_shr:4 row_mask:0xf bank_mask:0x2\n\t"
    "v_mfma_f32_16x16x32_bf16 v[88:91], v[220:223], v[164:167], v[88:91]\n\t"
    "ds_read_b128 v[212:215], v3 offset:25600\n\t"
    "v_mfma_f32_16x16x32_bf16 v[60:63], v[224:227], v[140:143], v[60:63]\n\t"
    "ds_read_b128 v[216:219], v53\n\t"
    "v_mfma_f32_16x16x32_bf16 v[64:67], v[228:231], v[140:143], v[64:67]\n\t"
    "ds_read_b128 v[220:223], v53 offset:1024\n\t"
    "s_waitcnt lgkmcnt(6)\n\t"
    "v_mfma_f32_16x16x32_bf16 v[60:63], v[184:187], v[120:123], v[60:63]\n\t"
    "s_mov_b32 exec_lo, 0x00ff00ff\n\t"
    "s_mov_b32 exec_hi, 0x00ff00ff\n\t"
    "ds_write_b128 v6, v[232:235]\n\t"
    "s_mov_b64 exec, -1\n\t"
    "v_add_u32 v6, s76, v6\n\t"
    "v_mfma_f32_16x16x32_bf16 v[84:87], v[184:187], v[168:171], v[84:87]\n\t"
    "ds_read_b128 v[224:227], v3 offset:30720\n\t"
    "v_mfma_f32_16x16x32_bf16 v[64:67], v[188:191], v[120:123], v[64:67]\n\t"
    "v_mfma_f32_16x16x32_bf16 v[88:91], v[188:191], v[168:171], v[88:91]\n\t"
    "ds_read_b128 v[228:231], v3 offset:31744\n\t"
    "v_mfma_f32_16x16x32_bf16 v[60:63], v[192:195], v[144:147], v[60:63]\n\t"
    "v_add_u32 v48, s55, v48\n\t"
    "ds_read_b128 v[184:187], v48\n\t"
    "v_mfma_f32_16x16x32_bf16 v[64:67], v[196:199], v[144:147], v[64:67]\n\t"
    "ds_read_b128 v[188:191], v48 offset:1024\n\t"
    "s_waitcnt lgkmcnt(7)\n\t"
    "v_mfma_f32_16x16x32_bf16 v[60:63], v[200:203], v[124:127], v[60:63]\n\t"
    "v_mfma_f32_16x16x32_bf16 v[84:87], v[200:203], v[172:175], v[84:87]\n\t"
    "v_add_u32 v3, s55, v3\n\t"
    "ds_read_b128 v[192:195], v3\n\t"
    "v_mfma_f32_16x16x32_bf16 v[64:67], v[204:207], v[124:127], v[64:67]\n\t"
    "v_mfma_f32_16x16x32_bf16 v[88:91], v[204:207], v[172:175], v[88:91]\n\t"
    "ds_read_b128 v[196:199], v3 offset:1024\n\t"
    "v_mfma_f32_16x16x32_bf16 v[60:63], v[208:211], v[148:151], v[60:63]\n\t"
    "v_add_u32 v49, s55, v49\n\t"
    "ds_read_b128 v[200:203], v49\n\t"
    "v_mfma_f32_16x16x32_bf16 v[64:67], v[212:215], v[148:151], v[64:67]\n\t"
    "ds_read_b128 v[204:207], v49 offset:1024\n\t"
    "s_waitcnt lgkmcnt(6)\n\t"
    "v_mfma_f32_16x16x32_bf16 v[60:63], v[216:219], v[128:131], v[60:63]\n\t"
    "v_mfma_f32_16x16x32_bf16 v[84:87], v[216:219], v[176:179], v[84:87]\n\t"
    "ds_read_b128 v[208:211], v3 offset:6144\n\t"
    "v_mfma_f32_16x16x32_bf16 v[64:67], v[220:223], v[128:131], v[64:67]\n\t"
    "v_mfma_f32_16x16x32_bf16 v[88:91], v[220:223], v[176:179], v[88:91]\n\t"
    "ds_read_b128 v[212:215], v3 offset:7168\n\t"
    "v_mfma_f32_16x16x32_bf16 v[60:63], v[224:227], v[152:155], v[60:63]\n\t"
    "v_add_u32 v50, s55, v50\n\t"
    "ds_read_b128 v[216:219], v50\n\t"
    "v_mfma_f32_16x16x32_bf16 v[64:67], v[228:231], v[152:155], v[64:67]\n\t"
    "ds_read_b128 v[220:223], v50 offset:1024\n\t"
    "s_waitcnt lgkmcnt(6)\n\t"
    "v_mfma_f32_16x16x32_bf16 v[68:71], v[184:187], v[108:111], v[68:71]\n\t"
    "v_mfma_f32_16x16x32_bf16 v[92:95], v[184:187], v[156:159], v[92:95]\n\t"
    "v_mfma_f32_16x16x32_bf16 v[232:235], v[184:187], v[180:183], 0\n\t"
    "ds_read_b128 v[224:227], v3 offset:12288\n\t"
    "v_mfma_f32_16x16x32_bf16 v[72:75], v[188:191], v[108:111], v[72:75]\n\t"
    "v_cvt_pk_bf16_f32 v240, v60, v61\n\t"
    "v_cvt_pk_bf16_f32 v241, v62, v63\n\t"
    "v_mfma_f32_16x16x32_bf16 v[96:99], v[188:191], v[156:159], v[96:99]\n\t"
    "v_cvt_pk_bf16_f32 v242, v64, v65\n\t"
    "v_cvt_pk_bf16_f32 v243, v66, v67\n\t"
    "v_mfma_f32_16x16x32_bf16 v[236:239], v[188:191], v[180:183], 0\n\t"
    "ds_read_b128 v[228:231], v3 offset:13312\n\t"
    "v_mfma_f32_16x16x32_bf16 v[68:71], v[192:195], v[132:135], v[68:71]\n\t"
    "v_add_u32 v51, s55, v51\n\t"
    "ds_read_b128 v[184:187], v51\n\t"
    "v_mfma_f32_16x16x32_bf16 v[72:75], v[196:199], v[132:135], v[72:75]\n\t"
    "ds_read_b128 v[188:191], v51 offset:1024\n\t"
    "s_waitcnt lgkmcnt(6)\n\t"
    "v_mfma_f32_16x16x32_bf16 v[68:71], v[200:203], v[112:115], v[68:71]\n\t"
    "v_permlane16_swap_b32 v240, v242\n\t"
    "v_permlane16_swap_b32 v241, v243\n\t"
    "v_mfma_f32_16x16x32_bf16 v[92:95], v[200:203], v[160:163], v[92:95]\n\t"
    "v_mfma_f32_16x16x32_bf16 v[72:75], v[204:207], v[112:115], v[72:75]\n\t"
    "buffer_store_dwordx4 v[240:243], v7, s[24:27], s56 offen offset:0 sc1\n\t"
    "v_mfma_f32_16x16x32_bf16 v[96:99], v[204:207], v[160:163], v[96:99]\n\t"
    "s_cmp_eq_u32 s37, 0\n\t"
    "s_cbranch_scc1 4f\n\t"
    "v_mfma_f32_16x16x32_bf16 v[232:235], v[200:203], v[16:19], v[232:235]\n\t"
    "v_mfma_f32_16x16x32_bf16 v[236:239], v[204:207], v[16:19], v[236:239]\n\t"
    "4:\n\t"
    "ds_read_b128 v[192:195], v3 offset:18432\n\t"
    "ds_read_b128 v[196:199], v3 offset:19456\n\t"
    "v_mfma_f32_16x16x32_bf16 v[68:71], v[208:211], v[136:139], v[68:71]\n\t"
    "v_add_u32 v52, s55, v52\n\t"
    "ds_read_b128 v[200:203], v52\n\t"
    "v_mfma_f32_16x16x32_bf16 v[72:75], v[212:215], v[136:139], v[72:75]\n\t"
    "ds_read_b128 v[204:207], v52 offset:1024\n\t"
    "s_setprio 2\n\t"
    "s_waitcnt lgkmcnt(6)\n\t"
    "v_mfma_f32_16x16x32_bf16 v[68:71], v[216:219], v[116:119], v[68:71]\n\t"
    "ds_write_b128 v4, v[84:87]\n\t"
    "v_mfma_f32_16x16x32_bf16 v[92:95], v[216:219], v[164:167], v[92:95]\n\t"
    "ds_read_b128 v[208:211], v3 offset:24576\n\t"
    "v_mfma_f32_16x16x32_bf16 v[72:75], v[220:223], v[116:119], v[72:75]\n\t"
    "v_mov_b32_dpp v232, v236 row_shr:4 row_mask:0xf bank_mask:0x2\n\t"
    "v_mov_b32_dpp v233, v237 row_shr:4 row_mask:0xf bank_mask:0x2\n\t"
    "v_mov_b32_dpp v234, v238 row_shr:4 row_mask:0xf bank_mask:0x2\n\t"
    "v_mov_b32_dpp v235, v239 row_shr:4 row_mask:0xf bank_mask:0x2\n\t"
    "v_mfma_f32_16x16x32_bf16 v[96:99], v[220:223], v[164:167], v[96:99]\n\t"
    "ds_read_b128 v[212:215], v3 offset:25600\n\t"
    "v_mfma_f32_16x16x32_bf16 v[68:71], v[224:227], v[140:143], v[68:71]\n\t"
    "v_add_u32 v53, s55, v53\n\t"
    "ds_read_b128 v[216:219], v53\n\t"
    "v_mfma_f32_16x16x32_bf16 v[72:75], v[228:231], v[140:143], v[72:75]\n\t"
    "ds_read_b128 v[220:223], v53 offset:1024\n\t"
    "s_waitcnt lgkmcnt(7)\n\t"
    "v_mfma_f32_16x16x32_bf16 v[68:71], v[184:187], v[120:123], v[68:71]\n\t"
    "ds_write_b128 v4, v[88:91] offset:1024\n\t"
    "v_mfma_f32_16x16x32_bf16 v[92:95], v[184:187], v[168:171], v[92:95]\n\t"
    "ds_read_b128 v[224:227], v3 offset:30720\n\t"
    "v_mfma_f32_16x16x32_bf16 v[72:75], v[188:191], v[120:123], v[72:75]\n\t"
    "s_mov_b32 exec_lo, 0x00ff00ff\n\t"
    "s_mov_b32 exec_hi, 0x00ff00ff\n\t"
    "ds_write_b128 v6, v[232:235]\n\t"
    "s_mov_b64 exec, -1\n\t"
    "v_add_u32 v6, s76, v6\n\t"
    "v_mfma_f32_16x16x32_bf16 v[96:99], v[188:191], v[168:171], v[96:99]\n\t"
    "ds_read_b128 v[228:231], v3 offset:31744\n\t"
    "v_mfma_f32_16x16x32_bf16 v[68:71], v[192:195], v[144:147], v[68:71]\n\t"
    "v_add_u32 v48, s55, v48\n\t"
    "ds_read_b128 v[184:187], v48\n\t"
    "v_mfma_f32_16x16x32_bf16 v[72:75], v[196:199], v[144:147], v[72:75]\n\t"
    "ds_read_b128 v[188:191], v48 offset:1024\n\t"
    "s_waitcnt lgkmcnt(8)\n\t"
    "v_mfma_f32_16x16x32_bf16 v[68:71], v[200:203], v[124:127], v[68:71]\n\t"
    "v_mfma_f32_16x16x32_bf16 v[92:95], v[200:203], v[172:175], v[92:95]\n\t"
    "v_add_u32 v3, s55, v3\n\t"
    "ds_read_b128 v[192:195], v3\n\t"
    "v_mfma_f32_16x16x32_bf16 v[72:75], v[204:207], v[124:127], v[72:75]\n\t"
    "v_mfma_f32_16x16x32_bf16 v[96:99], v[204:207], v[172:175], v[96:99]\n\t"
    "ds_read_b128 v[196:199], v3 offset:1024\n\t"
    "v_mfma_f32_16x16x32_bf16 v[68:71], v[208:211], v[148:151], v[68:71]\n\t"
    "v_add_u32 v49, s55, v49\n\t"
    "ds_read_b128 v[200:203], v49\n\t"
    "v_mfma_f32_16x16x32_bf16 v[72:75], v[212:215], v[148:151], v[72:75]\n\t"
    "ds_read_b128 v[204:207], v49 offset:1024\n\t"
    "s_waitcnt lgkmcnt(6)\n\t"
    "v_mfma_f32_16x16x32_bf16 v[68:71], v[216:219], v[128:131], v[68:71]\n\t"
    "v_mfma_f32_16x16x32_bf16 v[92:95], v[216:219], v[176:179], v[92:95]\n\t"
    "ds_read_b128 v[208:211], v3 offset:6144\n\t"
    "v_mfma_f32_16x16x32_bf16 v[72:75], v[220:223], v[128:131], v[72:75]\n\t"
    "v_mfma_f32_16x16x32_bf16 v[96:99], v[220:223], v[176:179], v[96:99]\n\t"
    "ds_read_b128 v[212:215], v3 offset:7168\n\t"
    "v_mfma_f32_16x16x32_bf16 v[68:71], v[224:227], v[152:155], v[68:71]\n\t"
    "v_add_u32 v50, s55, v50\n\t"
    "ds_read_b128 v[216:219], v50\n\t"
    "v_mfma_f32_16x16x32_bf16 v[72:75], v[228:231], v[152:155], v[72:75]\n\t"
    "ds_read_b128 v[220:223], v50 offset:1024\n\t"
    "s_waitcnt lgkmcnt(6)\n\t"
    "v_mfma_f32_16x16x32_bf16 v[76:79], v[184:187], v[108:111], v[76:79]\n\t"
    "v_mfma_f32_16x16x32_bf16 v[100:103], v[184:187], v[156:159], v[100:103]\n\t"
    "s_cmp_eq_u32 s38, 0\n\t"
    "s_cbranch_scc0 2f\n\t"
    "ds_write_b128 v4, v[96:99] offset:2048\n\t"
    "s_branch 3f\n\t"
    "2:\n\t"
    "ds_write_b128 v4, v[92:95] offset:2048\n\t"
    "3:\n\t"
    "ds_write_b32 v15, v15\n\t"
    "v_mfma_f32_16x16x32_bf16 v[232:235], v[184:187], v[180:183], 0\n\t"
    "v_add_u32 v51, s55, v51\n\t"
    "ds_read_b128 v[224:227], v51\n\t"
    "v_mfma_f32_16x16x32_bf16 v[80:83], v[188:191], v[108:111], v[80:83]\n\t"
    "v_cvt_pk_bf16_f32 v244, v68, v69\n\t"
    "v_cvt_pk_bf16_f32 v245, v70, v71\n\t"
    "v_mfma_f32_16x16x32_bf16 v[104:107], v[188:191], v[156:159], v[104:107]\n\t"
    "v_cvt_pk_bf16_f32 v246, v72, v73\n\t"
    "v_cvt_pk_bf16_f32 v247, v74, v75\n\t"
    "v_mfma_f32_16x16x32_bf16 v[236:239], v[188:191], v[180:183], 0\n\t"
    "ds_read_b128 v[228:231], v51 offset:1024\n\t"
    "v_mfma_f32_16x16x32_bf16 v[76:79], v[192:195], v[132:135], v[76:79]\n\t"
    "v_add_u32 v52, s55, v52\n\t"
    "ds_read_b128 v[184:187], v52\n\t"
    "v_mfma_f32_16x16x32_bf16 v[80:83], v[196:199], v[132:135], v[80:83]\n\t"
    "ds_read_b128 v[188:191], v52 offset:1024\n\t"
    "s_waitcnt lgkmcnt(8)\n\t"
    "v_mfma_f32_16x16x32_bf16 v[76:79], v[200:203], v[112:115], v[76:79]\n\t"
    "v_permlane16_swap_b32 v244, v246\n\t"
    "v_permlane16_swap_b32 v245, v247\n\t"
    "v_mfma_f32_16x16x32_bf16 v[100:103], v[200:203], v[160:163], v[100:103]\n\t"
    "v_mfma_f32_16x16x32_bf16 v[80:83], v[204:207], v[112:115], v[80:83]\n\t"
    "buffer_store_dwordx4 v[244:247], v7, s[24:27], s43 offen offset:64 sc1\n\t"
    "v_mfma_f32_16x16x32_bf16 v[104:107], v[204:207], v[160:163], v[104:107]\n\t"
    "s_cmp_eq_u32 s37, 0\n\t"
    "s_cbranch_scc1 4f\n\t"
    "v_mfma_f32_16x16x32_bf16 v[232:235], v[200:203], v[16:19], v[232:235]\n\t"
    "v_mfma_f32_16x16x32_bf16 v[236:239], v[204:207], v[16:19], v[236:239]\n\t"
    "4:\n\t"
    "v_add_u32 v53, s55, v53\n\t"
    "ds_read_b128 v[192:195], v53\n\t"
    "ds_read_b128 v[196:199], v53 offset:1024\n\t"
    "s_setprio 1\n\t"
    "v_mfma_f32_16x16x32_bf16 v[76:79], v[208:211], v[136:139], v[76:79]\n\t"
    "ds_read_b128 v[200:203], v3 offset:12288\n\t"
    "v_mfma_f32_16x16x32_bf16 v[80:83], v[212:215], v[136:139], v[80:83]\n\t"
    "ds_read_b128 v[204:207], v3 offset:13312\n\t"
    "s_waitcnt lgkmcnt(6)\n\t"
    "v_mfma_f32_16x16x32_bf16 v[76:79], v[216:219], v[116:119], v[76:79]\n\t"
    "v_mov_b32_dpp v232, v236 row_shr:4 row_mask:0xf bank_mask:0x2\n\t"
    "v_mov_b32_dpp v233, v237 row_shr:4 row_mask:0xf bank_mask:0x2\n\t"
    "v_mov_b32_dpp v234, v238 row_shr:4 row_mask:0xf bank_mask:0x2\n\t"
    "v_mov_b32_dpp v235, v239 row_shr:4 row_mask:0xf bank_mask:0x2\n\t"
    "v_mfma_f32_16x16x32_bf16 v[100:103], v[216:219], v[164:167], v[100:103]\n\t"
    "ds_read_b128 v[208:211], v3 offset:18432\n\t"
    "v_mfma_f32_16x16x32_bf16 v[80:83], v[220:223], v[116:119], v[80:83]\n\t"
    "s_mov_b32 exec_lo, 0x00ff00ff\n\t"
    "s_mov_b32 exec_hi, 0x00ff00ff\n\t"
    "ds_write_b128 v6, v[232:235]\n\t"
    "s_mov_b64 exec, -1\n\t"
    "ds_write_b32 v15, v15 offset:32\n\t"
    "v_mfma_f32_16x16x32_bf16 v[104:107], v[220:223], v[164:167], v[104:107]\n\t"
    "ds_read_b128 v[212:215], v3 offset:19456\n\t"
    "v_mfma_f32_16x16x32_bf16 v[76:79], v[224:227], v[120:123], v[76:79]\n\t"
    "v_mfma_f32_16x16x32_bf16 v[100:103], v[224:227], v[168:171], v[100:103]\n\t"
    "ds_read_b128 v[216:219], v3 offset:24576\n\t"
    "v_mfma_f32_16x16x32_bf16 v[80:83], v[228:231], v[120:123], v[80:83]\n\t"
    "ds_read_b32 v42, v2\n\t"
    "ds_read_b128 v[28:31], v5\n\t"
    "ds_read_b128 v[32:35], v5 offset:1024\n\t"
    "ds_read_b128 v[36:39], v5 offset:2048\n\t"
    "v_mfma_f32_16x16x32_bf16 v[104:107], v[228:231], v[168:171], v[104:107]\n\t"
    "ds_read_b128 v[220:223], v3 offset:25600\n\t"
    "s_setprio 0\n\t"
    "s_waitcnt lgkmcnt(12)\n\t"
    "v_mfma_f32_16x16x32_bf16 v[76:79], v[184:187], v[124:127], v[76:79]\n\t"
    "v_mfma_f32_16x16x32_bf16 v[100:103], v[184:187], v[172:175], v[100:103]\n\t"
    "ds_read_b128 v[224:227], v3 offset:30720\n\t"
    "v_mfma_f32_16x16x32_bf16 v[80:83], v[188:191], v[124:127], v[80:83]\n\t"
    "s_waitcnt lgkmcnt(2)\n\t"
    "s_mov_b32 s79, 0\n\t"
    "31:\n\t"
    "v_cmp_eq_u32 vcc, 0, v42\n\t"
    "s_add_u32 s79, s79, 1\n\t"
    "s_cbranch_vccz 32f\n\t"
    "s_cmp_gt_u32 s79, 262144\n\t"
    "s_cbranch_scc1 32f\n\t"
    "ds_read_b32 v42, v2\n\t"
    "ds_read_b128 v[28:31], v5\n\t"
    "ds_read_b128 v[32:35], v5 offset:1024\n\t"
    "ds_read_b128 v[36:39], v5 offset:2048\n\t"
    "s_waitcnt lgkmcnt(0)\n\t"
    "s_branch 31b\n\t"
    "32:\n\t"
    "s_cmp_eq_u32 s38, 0\n\t"
    "s_cbranch_scc0 27f\n\t"
    "v_add_f32 v36, v36, v92\n\t"
    "v_add_f32 v37, v37, v93\n\t"
    "v_add_f32 v38, v38, v94\n\t"
    "v_add_f32 v39, v39, v95\n\t"
    "s_branch 28f\n\t"
    "27:\n\t"
    "v_add_f32 v36, v36, v96\n\t"
    "v_add_f32 v37, v37, v97\n\t"
    "v_add_f32 v38, v38, v98\n\t"
    "v_add_f32 v39, v39, v99\n\t"
    "28:\n\t"
    "v_cvt_pk_bf16_f32 v40, v36, v37\n\t"
    "v_cvt_pk_bf16_f32 v41, v38, v39\n\t"
    "v_mfma_f32_16x16x32_bf16 v[104:107], v[188:191], v[172:175], v[104:107]\n\t"
    "ds_read_b128 v[228:231], v3 offset:31744\n\t"
    "v_mfma_f32_16x16x32_bf16 v[76:79], v[192:195], v[128:131], v[76:79]\n\t"
    "buffer_store_dwordx2 v[40:41], v9, s[24:27], s43 offen offset:64 sc1\n\t"
    "v_mfma_f32_16x16x32_bf16 v[100:103], v[192:195], v[176:179], v[100:103]\n\t"
    "v_mfma_f32_16x16x32_bf16 v[80:83], v[196:199], v[128:131], v[80:83]\n\t"
    "v_mfma_f32_16x16x32_bf16 v[104:107], v[196:199], v[176:179], v[104:107]\n\t"
    "v_mfma_f32_16x16x32_bf16 v[76:79], v[200:203], v[140:143], v[76:79]\n\t"
    "v_mfma_f32_16x16x32_bf16 v[80:83], v[204:207], v[140:143], v[80:83]\n\t"
    "v_mfma_f32_16x16x32_bf16 v[76:79], v[208:211], v[144:147], v[76:79]\n\t"
    "s_nop 2\n\t"
    "v_add_f32 v100, v100, v28\n\t"
    "v_add_f32 v101, v101, v29\n\t"
    "v_add_f32 v102, v102, v30\n\t"
    "v_add_f32 v103, v103, v31\n\t"
    "v_mfma_f32_16x16x32_bf16 v[80:83], v[212:215], v[144:147], v[80:83]\n\t"
    "v_add_f32 v104, v104, v32\n\t"
    "v_add_f32 v105, v105, v33\n\t"
    "v_add_f32 v106, v106, v34\n\t"
    "v_add_f32 v107, v107, v35\n\t"
    "v_cvt_pk_bf16_f32 v252, v100, v101\n\t"
    "v_cvt_pk_bf16_f32 v253, v102, v103\n\t"
    "s_waitcnt lgkmcnt(0)\n\t"
    "v_mfma_f32_16x16x32_bf16 v[76:79], v[216:219], v[148:151], v[76:79]\n\t"
    "v_cvt_pk_bf16_f32 v254, v104, v105\n\t"
    "v_cvt_pk_bf16_f32 v255, v106, v107\n\t"
    "v_mfma_f32_16x16x32_bf16 v[80:83], v[220:223], v[148:151], v[80:83]\n\t"
    "v_permlane16_swap_b32 v252, v254\n\t"
    "v_permlane16_swap_b32 v253, v255\n\t"
    "buffer_store_dwordx4 v[252:255], v8, s[24:27], s57 offen offset:0 sc1\n\t"
    "v_mfma_f32_16x16x32_bf16 v[76:79], v[224:227], v[152:155], v[76:79]\n\t"
    "s_cmp_gt_u32 s36, 3\n\t"
    "s_cbranch_scc1 38f\n\t"
    "v_and_b32 v108, 7, v22\n\t"
    "v_lshlrev_b32 v108, 2, v108\n\t"
    "v_add_u32 v108, 146976, v108\n\t"
    "s_mov_b32 s79, 0\n\t"
    "33:\n\t"
    "ds_read_b32 v109, v108\n\t"
    "ds_read_b128 v[112:115], v55\n\t"
    "ds_read_b128 v[116:119], v55 offset:64\n\t"
    "ds_read_b128 v[120:123], v55 offset:6144\n\t"
    "ds_read_b128 v[124:127], v55 offset:6208\n\t"
    "ds_read_b64 v[128:129], v56\n\t"
    "v_lshlrev_b32 v110, 16, v57\n\t"
    "v_and_b32 v111, 0xffff0000, v57\n\t"
    "s_waitcnt lgkmcnt(0)\n\t"
    "v_cmp_eq_u32 vcc, 0, v109\n\t"
    "s_add_u32 s79, s79, 1\n\t"
    "s_cbranch_vccz 34f\n\t"
    "s_cmp_le_u32 s79, 262144\n\t"
    "s_cbranch_scc1 33b\n\t"
    "34:\n\t"
    "v_add_f32 v112, v112, v120\n\t"
    "v_add_f32 v113, v113, v121\n\t"
    "v_add_f32 v114, v114, v122\n\t"
    "v_add_f32 v115, v115, v123\n\t"
    "v_add_f32 v116, v116, v124\n\t"
    "v_add_f32 v117, v117, v125\n\t"
    "v_add_f32 v118, v118, v126\n\t"
    "v_add_f32 v119, v119, v127\n\t"
    "v_permlane16_swap_b32 v112, v114\n\t"
    "v_permlane16_swap_b32 v113, v115\n\t"
    "v_permlane16_swap_b32 v116, v118\n\t"
    "v_permlane16_swap_b32 v117, v119\n\t"
    "v_add_f32 v112, v112, v114\n\t"
    "v_add_f32 v113, v113, v115\n\t"
    "v_add_f32 v116, v116, v118\n\t"
    "v_add_f32 v117, v117, v119\n\t"
    "s_nop 0\n\t"
    "v_permlane32_swap_b32 v112, v116\n\t"
    "v_permlane32_swap_b32 v113, v117\n\t"
    "v_add_f32 v112, v112, v116\n\t"
    "v_add_f32 v110, v110, v128\n\t"
    "v_add_f32 v112, v112, v110\n\t"
    "v_add_f32 v113, v113, v117\n\t"
    "v_add_f32 v111, v111, v129\n\t"
    "v_add_f32 v113, v113, v111\n\t"
    "v_cvt_pk_bf16_f32 v57, v112, v113\n\t"
    "s_mov_b32 exec_lo, 0x0fff0fff\n\t"
    "s_mov_b32 exec_hi, 0x0fff0fff\n\t"
    "buffer_store_dword v57, v54, s[24:27], s43 offen sc1\n\t"
    "s_mov_b64 exec, -1\n\t"
    "38:\n\t"
    "v_mfma_f32_16x16x32_bf16 v[80:83], v[228:231], v[152:155], v[80:83]\n\t"
    "s_nop 4\n\t"
    "v_cvt_pk_bf16_f32 v248, v76, v77\n\t"
    "v_cvt_pk_bf16_f32 v249, v78, v79\n\t"
    "s_nop 0\n\t"
    "v_cvt_pk_bf16_f32 v250, v80, v81\n\t"
    "v_cvt_pk_bf16_f32 v251, v82, v83\n\t"
    "s_nop 0\n\t"
    "v_permlane16_swap_b32 v248, v250\n\t"
    "v_permlane16_swap_b32 v249, v251\n\t"
    "buffer_store_dwordx4 v[248:251], v7, s[24:27], s57 offen offset:0 sc1\n\t"
      : "+{v0}"(tid)
      : "{s[0:1]}"(__builtin_amdgcn_kernarg_segment_ptr()), "{s2}"(blockIdx.x)
      : "v1", "v2", "v3", "v4", "v5", "v6", "v7", "v8", "v9", "v10", "v11", "v12", "v13", "v14", "v15", "v16", "v17", "v18", "v19", "v20", "v21", "v22", "v23", "v24", "v25", "v26", "v27", "v28", "v29", "v30", "v31", "v32", "v33", "v34", "v35", "v36", "v37", "v38", "v39", "v40", "v41", "v42", "v43", "v44", "v45", "v46", "v47", "v48", "v49", "v50", "v51", "v52", "v53", "v54", "v55", "v56", "v57", "v58", "v59", "v60", "v61", "v62", "v63", "v64", "v65", "v66", "v67", "v68", "v69", "v70", "v71", "v72", "v73", "v74", "v75", "v76", "v77", "v78", "v79", "v80", "v81", "v82", "v83", "v84", "v85", "v86", "v87", "v88", "v89", "v90", "v91", "v92", "v93", "v94", "v95", "v96", "v97", "v98", "v99", "v100", "v101", "v102", "v103", "v104", "v105", "v106", "v107", "v108", "v109", "v110", "v111", "v112", "v113", "v114", "v115", "v116", "v117", "v118", "v119", "v120", "v121", "v122", "v123", "v124", "v125", "v126", "v127", "v128", "v129", "v130", "v131", "v132", "v133", "v134", "v135", "v136", "v137", "v138", "v139", "v140", "v141", "v142", "v143", "v144", "v145", "v146", "v147", "v148", "v149", "v150", "v151", "v152", "v153", "v154", "v155", "v156", "v157", "v158", "v159", "v160", "v161", "v162", "v163", "v164", "v165", "v166", "v167", "v168", "v169", "v170", "v171", "v172", "v173", "v174", "v175", "v176", "v177", "v178", "v179", "v180", "v181", "v182", "v183", "v184", "v185", "v186", "v187", "v188", "v189", "v190", "v191", "v192", "v193", "v194", "v195", "v196", "v197", "v198", "v199", "v200", "v201", "v202", "v203", "v204", "v205", "v206", "v207", "v208", "v209", "v210", "v211", "v212", "v213", "v214", "v215", "v216", "v217", "v218", "v219", "v220", "v221", "v222", "v223", "v224", "v225", "v226", "v227", "v228", "v229", "v230", "v231", "v232", "v233", "v234", "v235", "v236", "v237", "v238", "v239", "v240", "v241", "v242", "v243", "v244", "v245", "v246", "v247", "v248", "v249", "v250", "v251", "v252", "v253", "v254", "v255", "s3", "s4", "s5", "s6", "s7", "s8", "s9", "s10", "s11", "s12", "s13", "s14", "s15", "s16", "s17", "s18", "s19", "s20", "s21", "s22", "s23", "s24", "s25", "s26", "s27", "s28", "s29", "s30", "s31", "s32", "s33", "s34", "s35", "s36", "s37", "s38", "s39", "s40", "s41", "s42", "s43", "s44", "s45", "s46", "s47", "s48", "s49", "s50", "s51", "s52", "s53", "s54", "s55", "s56", "s57", "s58", "s59", "s60", "s61", "s62", "s63", "s64", "s65", "s66", "s67", "s68", "s69", "s70", "s71", "s72", "s73", "s74", "s75", "s76", "s77", "s78", "s79", "s80", "s81", "s82", "s83", "s84", "s85", "s86", "s87", "s88", "s89", "vcc", "m0", "scc", "memory");
}

extern "C" void kernel_launch(void* const* d_in, const int* in_sizes, int n_in, void* d_out, int out_size, void* d_ws, size_t ws_size,
                              hipStream_t stream) {
  (void)hipFuncSetAttribute((const void*)convblock_kernel, hipFuncAttributeMaxDynamicSharedMemorySize, LDS_BYTES);
  convblock_kernel<<<256, 512, LDS_BYTES, stream>>>(d_in[0], d_in[1], d_in[2], d_in[3], d_in[4], d_in[5], d_out, d_ws);
}
```
